```python
import torch

def _softmax(x, axis=-1):
    return torch.softmax(x, dim=axis)
def _gelu(x):
    return torch.nn.functional.gelu(x, approximate="tanh")
def _silu(x):
    return torch.nn.functional.silu(x)
def _sigmoid(x):
    return torch.sigmoid(x)
def _layernorm(x, axis=-1, eps=1e-5):
    m = x.mean(dim=axis, keepdim=True)
    v = x.var(dim=axis, keepdim=True, unbiased=False)
    return (x - m) / torch.sqrt(v + eps)
def _scatter_add(tgt, idx, src, axis=0):
    return tgt.clone().index_add_(axis, idx, src)
def _segment_sum(src, seg, n):
    out = torch.zeros((n,) + src.shape[1:], dtype=src.dtype)
    return out.index_add_(0, seg, src)
def _pad(x, pw):
    flat = tuple(v for pair in reversed(pw) for v in pair)
    return torch.nn.functional.pad(x, flat)

kernel_name = 'standalone__mixer_channel_mlp__prompt'

def setup_inputs():

    _w = (lambda a: (lambda t: t.to(torch.bfloat16) if t.is_floating_point() else t)(torch.as_tensor(a)))
    B, S, D, F = 1, 2048, 512, 1792
    return {
        "x": _w((lambda *s: torch.randn(*s, dtype=torch.bfloat16) * 0.05)(B, S, D)),
        "w1": _w((lambda *s: torch.randn(*s, dtype=torch.bfloat16) * 0.8)(D, F) * (D ** -0.5)),
        "b1": _w((lambda *s: torch.randn(*s, dtype=torch.bfloat16) * 0.4)(F)),
        "w2": _w((lambda *s: torch.randn(*s, dtype=torch.bfloat16) * 0.05)(F, D) * (F ** -0.5)),
        "b2": _w((lambda *s: torch.randn(*s, dtype=torch.bfloat16) * 0.05)(D)),
    }

def reference(x, w1, b1, w2, b2):
    y = _layernorm(x, axis=-1)
    h = _gelu(torch.einsum("bsd,df->bsf", y, w1) + b1)
    return torch.einsum("bsf,fd->bsd", h, w2) + b2
```

```cpp
#include <hip/hip_runtime.h>
#include <hip/hip_bf16.h>
#include <stdint.h>
typedef short bf16x8 __attribute__((ext_vector_type(8)));
typedef float f32x4 __attribute__((ext_vector_type(4)));
typedef unsigned short u16;

constexpr int S = 2048, D = 512, F = 1792;

__device__ __forceinline__ float bf2f(u16 b) { return __builtin_bit_cast(float, (unsigned)b << 16); }
__device__ __forceinline__ u16 f2bf(float f) { return __builtin_bit_cast(u16, (__bf16)f); }

__global__ void ln_kernel(const u16* x, u16* y) {
  int row = blockIdx.x * 4 + (threadIdx.x >> 6), lane = threadIdx.x & 63;
  bf16x8 v = *(const bf16x8*)(x + row * D + lane * 8);
  float f[8], s = 0.f;
  for (int j = 0; j < 8; ++j) { f[j] = bf2f(v[j]); s += f[j]; }
  for (int o = 32; o; o >>= 1) s += __shfl_xor(s, o);
  float m = s * (1.f / D), q = 0.f;
  for (int j = 0; j < 8; ++j) { f[j] -= m; q += f[j] * f[j]; }
  for (int o = 32; o; o >>= 1) q += __shfl_xor(q, o);
  float r = rsqrtf(q * (1.f / D) + 1e-5f);
  bf16x8 o8;
  for (int j = 0; j < 8; ++j) o8[j] = f2bf(f[j] * r);
  *(bf16x8*)(y + row * D + lane * 8) = o8;
}

template <int K, int N, bool GELU>
__global__ __launch_bounds__(256) void gemm_kernel(const u16* A, const u16* B, const u16* bias, u16* C) {
  __shared__ u16 As[64][40];
  __shared__ u16 Bs[64][40];
  int bm = blockIdx.y * 64, bn = blockIdx.x * 64;
  int tid = threadIdx.x, lane = tid & 63, w = tid >> 6, wr = w >> 1, wc = w & 1;
  f32x4 acc[2][2] = {};
  for (int k0 = 0; k0 < K; k0 += 32) {
    {
      int r = tid >> 2, c = (tid & 3) * 8;
      *(bf16x8*)&As[r][c] = *(const bf16x8*)(A + (bm + r) * K + k0 + c);
      int kk = tid >> 3, n = (tid & 7) * 8;
      bf16x8 b = *(const bf16x8*)(B + (k0 + kk) * N + bn + n);
      for (int j = 0; j < 8; ++j) Bs[n + j][kk] = b[j];
    }
    __syncthreads();
    bf16x8 a[2], b[2];
    for (int i = 0; i < 2; ++i) {
      a[i] = *(const bf16x8*)&As[wr * 32 + i * 16 + (lane & 15)][(lane >> 4) * 8];
      b[i] = *(const bf16x8*)&Bs[wc * 32 + i * 16 + (lane & 15)][(lane >> 4) * 8];
    }
    for (int i = 0; i < 2; ++i)
      for (int j = 0; j < 2; ++j) acc[i][j] = __builtin_amdgcn_mfma_f32_16x16x32_bf16(a[i], b[j], acc[i][j], 0, 0, 0);
    __syncthreads();
  }
  for (int i = 0; i < 2; ++i)
    for (int j = 0; j < 2; ++j)
      for (int r = 0; r < 4; ++r) {
        int row = bm + wr * 32 + i * 16 + (lane >> 4) * 4 + r, col = bn + wc * 32 + j * 16 + (lane & 15);
        float v = acc[i][j][r] + bf2f(bias[col]);
        if (GELU) {
          float u = 0.7978845608028654f * (v + 0.044715f * v * v * v);
          v = v / (1.f + __expf(-2.f * u));
        }
        C[row * N + col] = f2bf(v);
      }
}

extern "C" void kernel_launch(void* const* d_in, const int* in_sizes, int n_in, void* d_out, int out_size, void* d_ws,
                              size_t ws_size, hipStream_t stream) {
  const u16* x = (const u16*)d_in[0];
  const u16* w1 = (const u16*)d_in[1];
  const u16* b1 = (const u16*)d_in[2];
  const u16* w2 = (const u16*)d_in[3];
  const u16* b2 = (const u16*)d_in[4];
  u16* y = (u16*)d_ws;
  u16* h = y + S * D;
  ln_kernel<<<S / 4, 256, 0, stream>>>(x, y);
  gemm_kernel<D, F, true><<<dim3(F / 64, S / 64), 256, 0, stream>>>(y, w1, b1, h);
  gemm_kernel<F, D, false><<<dim3(D / 64, S / 64), 256, 0, stream>>>(h, w2, b2, (u16*)d_out);
}
```

```cpp
#include <hip/hip_runtime.h>
#include <stdint.h>

typedef unsigned u32;
typedef unsigned v4u __attribute__((ext_vector_type(4)));
typedef unsigned v2u __attribute__((ext_vector_type(2)));
typedef short s16x4 __attribute__((ext_vector_type(4)));
typedef short s16x8 __attribute__((ext_vector_type(8)));
typedef __bf16 bf16x2 __attribute__((ext_vector_type(2)));
typedef __bf16 bf16x8 __attribute__((ext_vector_type(8)));
typedef float f32x4 __attribute__((ext_vector_type(4)));
typedef float f32x16 __attribute__((ext_vector_type(16)));
#define LAS __attribute__((address_space(3)))
#define DI __device__ __forceinline__

namespace {
constexpr int D = 512, F = 1792;
constexpr u32 WS_CTL = 0;
constexpr u32 WS_STAT = 65536;
constexpr u32 WS_H = 1u << 20;
constexpr u32 WS_P = 9u << 20;
constexpr u32 WS_BYTES = 14u << 20;
constexpr u32 L_SLOT = 24576;
constexpr u32 L_XO = 90112;
constexpr u32 L_RED = 114688;
constexpr u32 L_CS = 155648;
constexpr u32 L_B1 = L_CS + 1024;
constexpr u32 L_B2 = L_B1 + 256;
constexpr u32 L_FLG = L_B2 + 256;
constexpr u32 L_BYTES = 163840;
constexpr u32 SPIN_CAP = 1u << 18;

DI v4u mk_rsrc(const void* p, u32 bytes) {
  uint64_t a = (uint64_t)p;
  v4u r;
  r.x = (u32)a; r.y = (u32)(a >> 32) & 0xffffu; r.z = bytes; r.w = 0x00020000u;
  return r;
}
DI void dma16(v4u rs, u32 voff, u32 soff, u32 lds) {
  asm volatile("s_mov_b32 m0, %3\n\ts_nop 0\n\tbuffer_load_dwordx4 %0, %1, %2 offen lds" ::"v"(voff), "s"(rs), "s"(soff), "s"(lds) : "memory");
}
DI void dma4(v4u rs, u32 voff, u32 soff, u32 lds) {
  asm volatile("s_mov_b32 m0, %3\n\ts_nop 0\n\tbuffer_load_dword %0, %1, %2 offen lds" ::"v"(voff), "s"(rs), "s"(soff), "s"(lds) : "memory");
}
DI void ld16_sc1(v4u& d, v4u rs, u32 voff, u32 soff) {
  asm volatile("buffer_load_dwordx4 %0, %1, %2, %3 offen sc1" : "=v"(d) : "v"(voff), "s"(rs), "s"(soff) : "memory");
}
DI void ld16(v4u& d, v4u rs, u32 voff, u32 soff) {
  asm volatile("buffer_load_dwordx4 %0, %1, %2, %3 offen" : "=v"(d) : "v"(voff), "s"(rs), "s"(soff) : "memory");
}
DI void ld4_sc1(u32& d, v4u rs, u32 voff, u32 soff) {
  asm volatile("buffer_load_dword %0, %1, %2, %3 offen sc1" : "=v"(d) : "v"(voff), "s"(rs), "s"(soff) : "memory");
}
DI void st16_sc1(v4u d, v4u rs, u32 voff, u32 soff) {
  asm volatile("buffer_store_dwordx4 %0, %1, %2, %3 offen sc1\n\ts_nop 1" ::"v"(d), "v"(voff), "s"(rs), "s"(soff) : "memory");
}
DI void st16(v4u d, v4u rs, u32 voff, u32 soff) {
  asm volatile("buffer_store_dwordx4 %0, %1, %2, %3 offen\n\ts_nop 1" ::"v"(d), "v"(voff), "s"(rs), "s"(soff) : "memory");
}
DI void st4_sc1(u32 d, v4u rs, u32 voff, u32 soff) {
  asm volatile("buffer_store_dword %0, %1, %2, %3 offen sc1" ::"v"(d), "v"(voff), "s"(rs), "s"(soff) : "memory");
}
template <int N> DI void wait_vm() { asm volatile("s_waitcnt vmcnt(%0)" ::"i"(N) : "memory"); }
template <int N, class T> DI void wait_vm(T& pin) { asm volatile("s_waitcnt vmcnt(%1)" : "+v"(pin) : "i"(N) : "memory"); }
DI void wait_lgkm0() { __builtin_amdgcn_s_waitcnt(0xC07F); asm volatile("" ::: "memory"); }
DI void bar() { asm volatile("s_barrier" ::: "memory"); }
DI void sched_fence() { __builtin_amdgcn_sched_barrier(0); }

DI bf16x8 lds_b128(u32 a) { return *(const LAS bf16x8*)a; }
DI f32x4 lds_f4(u32 a) { return *(const LAS f32x4*)a; }
DI bf16x8 lds_tr(u32 a0, u32 a1) {
  s16x4 lo = __builtin_amdgcn_ds_read_tr16_b64_v4i16((LAS s16x4*)a0);
  s16x4 hi = __builtin_amdgcn_ds_read_tr16_b64_v4i16((LAS s16x4*)a1);
  return __builtin_bit_cast(bf16x8, __builtin_shufflevector(lo, hi, 0, 1, 2, 3, 4, 5, 6, 7));
}
DI f32x16 mfma32(bf16x8 a, bf16x8 b, f32x16 c) { return __builtin_amdgcn_mfma_f32_32x32x16_bf16(a, b, c, 0, 0, 0); }
DI f32x4 mfma16(bf16x8 a, bf16x8 b, f32x4 c) { return __builtin_amdgcn_mfma_f32_16x16x32_bf16(a, b, c, 0, 0, 0); }
DI u32 pack2(float a, float b) { bf16x2 v = {(__bf16)a, (__bf16)b}; return __builtin_bit_cast(u32, v); }
DI float asf(u32 w) { return __builtin_bit_cast(float, w); }
DI u32 asu(float f) { return __builtin_bit_cast(u32, f); }
DI bf16x2 asb2(u32 w) { return __builtin_bit_cast(bf16x2, w); }
DI float bf_lo(u32 w) { return __builtin_bit_cast(float, w << 16); }
DI float bf_hi(u32 w) { return __builtin_bit_cast(float, w & 0xffff0000u); }

DI float gelu1(float acc, float cs, float b1, float nm, float rs) {
  const float c1 = -2.3022081986f, c2 = -0.1029432397f;
  float t = __builtin_fmaf(nm, cs, acc);
  float v = __builtin_fmaf(t, rs, b1);
  float p = __builtin_fmaf(v * v, c2, c1);
  float e = __builtin_amdgcn_exp2f(v * p);
  return v * __builtin_amdgcn_rcpf(1.0f + e);
}
DI void gelu4(const f32x16& acc, int q, u32 csa, u32 b1a, float nm, float rs, v4u& o) {
  f32x4 c0 = lds_f4(csa + q * 32), c1 = lds_f4(csa + 512 + q * 32);
  v2u b = *(const LAS v2u*)(b1a + q * 16);
  float g0 = gelu1(acc[4 * q + 0], c0[0] + c1[0], bf_lo(b.x), nm, rs);
  float g1 = gelu1(acc[4 * q + 1], c0[1] + c1[1], bf_hi(b.x), nm, rs);
  float g2 = gelu1(acc[4 * q + 2], c0[2] + c1[2], bf_lo(b.y), nm, rs);
  float g3 = gelu1(acc[4 * q + 3], c0[3] + c1[3], bf_hi(b.y), nm, rs);
  o[2 * (q & 1)] = pack2(g0, g1); o[2 * (q & 1) + 1] = pack2(g2, g3);
}
#define STAMP(i)
}

__global__ __launch_bounds__(512, 2) void mixer_mlp(const char* x, const char* w1, const char* b1,
                                                    const char* w2, const char* b2, char* out,
                                                    char* ws) {
  extern __shared__ __attribute__((aligned(16))) char smem[];
  STAMP(0);
  asm volatile("" : "+s"(x), "+s"(w1), "+s"(b1), "+s"(w2), "+s"(b2), "+s"(out), "+s"(ws));
  const u32 lane = threadIdx.x & 63;
  const u32 w = __builtin_amdgcn_readfirstlane(threadIdx.x >> 6);
  const u32 xcd = blockIdx.x & 7, idx = blockIdx.x >> 3;
  const u32 kh = xcd & 1, panel = (xcd >> 1) * 4 + (idx & 3), m = idx >> 2;
  const u32 pk = panel * 2 + kh;
  const v4u rws = mk_rsrc(ws, WS_BYTES);
  const v4u rx = mk_rsrc(x + panel * (128 * D * 2), 128 * D * 2);
  const v4u rw2 = mk_rsrc(w2, F * D * 2);
  const u32 t31 = lane & 31, h = lane >> 5;
  const u32 rowF = (1 - kh) * 64 * 1024, rowO = kh * 64 * 1024;
  const u32 ctl = WS_CTL + panel * 4096;
  const u32 flg = ctl + 512 + kh * 1536;

  u32 ep;
  ld4_sc1(ep, rws, 0, ctl);
  if (lane < 8) *(LAS u32*)(L_FLG + lane * 4) = 0;
  if (w == 0) dma4(mk_rsrc(b2, D * 2), lane * 4, m * 128, L_B2);
  const u32 w2v = (8 * w + (lane >> 3)) * 1024 + (((lane & 7) ^ ((lane >> 2) & 4)) * 16);
  const u32 w2s = kh * 896 * 1024 + m * 128;
  auto dma_w2 = [&](int i) { dma16(rw2, w2v, w2s + i * 65536, i * 8192 + w * 1024); };
  const u32 xrow = 8 * w + (lane >> 3);
  u32 tag;

  if (m == 0) {
    v4u xr[2][8];
    const u32 xv = xrow * 1024 + (lane & 7) * 16;
#pragma unroll
    for (int s = 0; s < 2; ++s)
#pragma unroll
      for (int i = 0; i < 8; ++i) ld16(xr[s][i], rx, xv, (s ? rowO : rowF) + i * 128);
    wait_vm<16>(ep);
    tag = __builtin_amdgcn_readfirstlane(ep) + 1;
#pragma unroll
    for (int s = 0; s < 2; ++s) {
      float sum = 0.f, sq = 0.f;
      const bf16x2 ones = {(__bf16)1.0f, (__bf16)1.0f};
#pragma unroll
      for (int i = 0; i < 8; ++i) {
        if (s == 0) { switch (i) { case 0: wait_vm<15>(xr[s][i]); break; case 1: wait_vm<14>(xr[s][i]); break; case 2: wait_vm<13>(xr[s][i]); break; case 3: wait_vm<12>(xr[s][i]); break;
                                   case 4: wait_vm<11>(xr[s][i]); break; case 5: wait_vm<10>(xr[s][i]); break; case 6: wait_vm<9>(xr[s][i]); break; default: wait_vm<8>(xr[s][i]); } }
        else { switch (i) { case 0: wait_vm<8>(xr[s][i]); break; case 1: wait_vm<7>(xr[s][i]); break; case 2: wait_vm<6>(xr[s][i]); break; case 3: wait_vm<5>(xr[s][i]); break;
                            case 4: wait_vm<4>(xr[s][i]); break; case 5: wait_vm<3>(xr[s][i]); break; case 6: wait_vm<2>(xr[s][i]); break; default: wait_vm<1>(xr[s][i]); } }
#pragma unroll
        for (int c = 0; c < 4; ++c) {
          bf16x2 v = asb2(xr[s][i][c]);
          sum = __builtin_amdgcn_fdot2_f32_bf16(v, ones, sum, false);
          sq = __builtin_amdgcn_fdot2_f32_bf16(v, v, sq, false);
        }
      }
#pragma unroll
      for (int o = 1; o < 8; o <<= 1) { sum += __shfl_xor(sum, o); sq += __shfl_xor(sq, o); }
      const float mean = sum * (1.0f / D);
      const float var = fmaxf(sq * (1.0f / D) - mean * mean, 0.f);
      const float rs = __builtin_amdgcn_rsqf(var + 1e-5f);
      v4u g = {__builtin_bit_cast(u32, -mean), tag, __builtin_bit_cast(u32, rs), tag};
      st16_sc1(g, rws, (lane & 7) ? 0xfffffff0u - WS_BYTES : (s * 64 + xrow) * 16, WS_STAT + pk * 2048);
    }
    STAMP(1);
#pragma unroll
    for (int i = 0; i < 14; ++i) dma_w2(i);
    wait_vm<0>();
    STAMP(6);
    bar();
  } else {
    const u32 j = m - 1, g = w & 1, ft = w >> 1;
    const v4u rw1 = mk_rsrc(w1, D * F * 2);
    const u32 f0 = kh * 896 + j * 128;
    if (w == 1) dma4(mk_rsrc(b1, F * 2), lane * 4, f0 * 2, L_B1);
    const u32 xv = xrow * 1024 + (((lane & 7) ^ ((xrow >> 1) & 7)) * 16);
    const u32 w1v = (8 * w + (lane >> 4)) * (F * 2) + (((lane & 15) ^ ((lane >> 4) << 2)) * 16);
    const u32 w1s = f0 * 2;
    auto dma_g = [&](int kg) {
      const u32 sl = (kg % 3) * L_SLOT;
      dma16(rx, xv, rowF + kg * 128, sl + w * 1024);
      dma16(rw1, w1v, w1s + kg * 64 * (F * 2), sl + 8192 + w * 2048);
      dma16(rw1, w1v, w1s + (kg * 64 + 4) * (F * 2), sl + 8192 + w * 2048 + 1024);
    };
    auto dma_xo = [&](int kg) { dma16(rx, xv, rowO + kg * 128, L_XO + kg * 8192 + w * 1024); };
    dma_g(0);
    dma_g(1);
    u32 xa[4];
#pragma unroll
    for (int k = 0; k < 4; ++k) xa[k] = (32 * g + t31) * 128 + (((2 * (k ^ (2 * g)) + h) ^ ((t31 >> 1) & 7)) * 16);
    const u32 q4 = (lane >> 2) & 3;
    const u32 wa = 8192 + (8 * h + q4) * 256 + ((ft ^ q4) * 64) + ((lane >> 4) & 1) * 32 + (lane & 3) * 8;
    const u32 wa2[2] = {wa + g * 8192, wa + 8192 - g * 8192};
    const u32 selw = ((lane & 15) == ((lane >> 4) & 1)) ? 0x3f803f80u : 0u;
    const bf16x8 sel = __builtin_bit_cast(bf16x8, v4u{selw, selw, selw, selw});
    bf16x8 wf[32], xf[2][4];
    f32x16 accF = {}, accO = {};
    f32x4 cs = {};
    v4u st0, st1;
    const u32 stv = (32 * g + t31) * 16;
#pragma unroll
    for (int kg = 0; kg < 8; ++kg) {
      wait_vm<3>();
      bar();
      if (kg == 0) STAMP(1);
      if (kg == 7) ld16_sc1(st0, rws, stv, WS_STAT + pk * 2048);
      if (kg < 6) dma_g(kg + 2);
      else { dma_xo(3 * (kg - 6)); dma_xo(3 * (kg - 6) + 1); dma_xo(3 * (kg - 6) + 2); }
      const u32 sl = (kg % 3) * L_SLOT;
#pragma unroll
      for (int k = 0; k < 4; ++k) {
        xf[kg & 1][k] = lds_b128(xa[k] + sl);
        wf[4 * kg + k] = lds_tr(wa2[k >> 1] + sl + (k & 1) * 4096, wa2[k >> 1] + sl + (k & 1) * 4096 + 1024);
      }
      if (kg > 0) {
#pragma unroll
        for (int k = 0; k < 4; ++k) accF = mfma32(wf[4 * (kg - 1) + k], xf[(kg - 1) & 1][k], accF);
        cs = mfma16(sel, wf[4 * (kg - 1) + 0], cs); cs = mfma16(sel, wf[4 * (kg - 1) + 1], cs);
      }
      wait_lgkm0();
      sched_fence();
    }
    cs = mfma16(sel, wf[28], cs); cs = mfma16(sel, wf[29], cs);
#pragma unroll
    for (int k = 0; k < 4; ++k) accF = mfma32(wf[28 + k], xf[1][k], accF);
    if (lane < 16) {
      *(LAS float*)(L_CS + g * 512 + ft * 128 + lane * 4) = cs[0];
      *(LAS float*)(L_CS + g * 512 + ft * 128 + 64 + lane * 4) = cs[1];
    }
    wait_lgkm0();
    wait_vm<3>(st0);
    asm volatile("" : "+v"(ep));
    tag = __builtin_amdgcn_readfirstlane(ep) + 1;
    for (u32 spin = 0; __builtin_amdgcn_ballot_w64(st0.y != tag || st0.w != tag) != 0 && spin < SPIN_CAP; ++spin) {
      ld16_sc1(st0, rws, stv, WS_STAT + pk * 2048);
      wait_vm<0>(st0);
    }
    bar();
    STAMP(2);
    const u32 csa = L_CS + ft * 128 + h * 16, b1a = L_B1 + ft * 64 + h * 8;
    const u32 hbase = WS_H + pk * (4 * 56 * 1024) + g * (56 * 1024) + (8 * j + 2 * ft) * 1024;
    const u32 lv16 = lane * 16;
    const u32 fidx = 4 * j + ft, fkk = fidx >= 14 ? 1 : 0;
    const u32 myflag = flg + g * 256 + fkk * 128 + (fidx - 14 * fkk) * 4;
    const u32 l0 = lane == 0 ? 0u : 0xfffffff0u - WS_BYTES;
    v4u ho;
    float nm = asf(st0.x), rs = asf(st0.z);
#pragma unroll
    for (int k = 0; k < 4; ++k) xf[0][k] = lds_b128(xa[k] + L_XO);
#pragma unroll
    for (int s = 0; s < 8; ++s) {
      if (s == 2) { wait_vm<4>(); bar(); }
      if (s == 5) { wait_vm<10>(); bar(); }
      if (s == 0) { dma_xo(6); dma_xo(7); }
      else if (s == 1) { dma_w2(9); dma_w2(10); }
      else if (s < 5) { dma_w2(2 * s - 4); dma_w2(2 * s - 3); }
      else if (s == 5) { dma_w2(6); dma_w2(7); ld16_sc1(st1, rws, stv + 1024, WS_STAT + pk * 2048); }
      else if (s == 6) { dma_w2(8); dma_w2(11); }
      else { dma_w2(12); dma_w2(13); }
      if (s < 7) {
#pragma unroll
        for (int k = 0; k < 4; ++k) xf[(s + 1) & 1][k] = lds_b128(xa[k] + L_XO + (s + 1) * 8192);
      }
#pragma unroll
      for (int k = 0; k < 4; ++k) accO = mfma32(wf[4 * s + k], xf[s & 1][k], accO);
      if (s == 0) gelu4(accF, 0, csa, b1a, nm, rs, ho);
      if (s == 2) { gelu4(accF, 1, csa, b1a, nm, rs, ho); st16_sc1(ho, rws, lv16, hbase); }
      if (s == 3) gelu4(accF, 2, csa, b1a, nm, rs, ho);
      if (s == 4) { gelu4(accF, 3, csa, b1a, nm, rs, ho); st16_sc1(ho, rws, lv16, hbase + 1024); }
      wait_lgkm0();
      sched_fence();
    }
    STAMP(3);
    wait_vm<7>();
    st4_sc1(tag, rws, l0, myflag);
    wait_vm<5>(st1);
    for (u32 spin = 0; __builtin_amdgcn_ballot_w64(st1.y != tag || st1.w != tag) != 0 && spin < SPIN_CAP; ++spin) {
      ld16_sc1(st1, rws, stv + 1024, WS_STAT + pk * 2048);
      wait_vm<0>(st1);
    }
    STAMP(4);
    nm = asf(st1.x); rs = asf(st1.z);
    gelu4(accO, 0, csa, b1a, nm, rs, ho);
    gelu4(accO, 1, csa, b1a, nm, rs, ho);
    st16_sc1(ho, rws, lv16, hbase + 2 * 56 * 1024);
    gelu4(accO, 2, csa, b1a, nm, rs, ho);
    gelu4(accO, 3, csa, b1a, nm, rs, ho);
    st16_sc1(ho, rws, lv16, hbase + 2 * 56 * 1024 + 1024);
    STAMP(5);
    wait_vm<3>();
    bar();
    wait_vm<0>();
    st4_sc1(tag, rws, l0, myflag + 512);
    STAMP(6);
  }

  {
    const u32 hh = w >> 2, g = w & 1, kk = (w >> 1) & 1;
    const u32 lv16 = lane * 16;
    {
      const u32 pv = (lane < 14 ? lane : 0) * 4;
      u32 fv;
      for (u32 spin = 0; spin < SPIN_CAP; ++spin) {
        ld4_sc1(fv, rws, pv, flg + hh * 512 + g * 256 + kk * 128);
        wait_vm<0>(fv);
        if (__builtin_amdgcn_ballot_w64(fv != tag) == 0) break;
      }
    }
    STAMP(7);
    const u32 hsrc = WS_H + pk * (4 * 56 * 1024) + (hh * 2 + g) * (56 * 1024) + kk * (28 * 1024);
    constexpr int WIN = 10;
    v4u hf[28];
#pragma unroll
    for (int i = 0; i < WIN; ++i) ld16_sc1(hf[i], rws, lv16, hsrc + i * 1024);
    const u32 q4 = (lane >> 2) & 3, p4 = lane & 3, blk = (lane >> 4) & 1;
    const u32 a0 = kk * 57344 + (4 * h + q4) * 128 + ((q4 >> 1) * 64) + (4 * (p4 & 1) + 2 * blk + (p4 >> 1)) * 8;
    const u32 a1 = a0 ^ 64;
    f32x16 acc0 = {}, acc1 = {};
    bf16x8 wa0[2], wa1[2];
    wa0[0] = lds_tr(a0, a0 + 1024);
    wa1[0] = lds_tr(a1, a1 + 1024);
#define G2STEP(i, N)                                                                   \
    {                                                                                   \
      if (i + 1 < 28) {                                                                 \
        wa0[(i + 1) & 1] = lds_tr(a0 + (i + 1) * 2048, a0 + (i + 1) * 2048 + 1024);     \
        wa1[(i + 1) & 1] = lds_tr(a1 + (i + 1) * 2048, a1 + (i + 1) * 2048 + 1024);     \
      }                                                                                 \
      wait_vm<N>(hf[i]);                                                                \
      acc0 = mfma32(wa0[i & 1], __builtin_bit_cast(bf16x8, hf[i]), acc0);               \
      acc1 = mfma32(wa1[i & 1], __builtin_bit_cast(bf16x8, hf[i]), acc1);               \
      if (i + WIN < 28) ld16_sc1(hf[(i + WIN) % 28], rws, lv16, hsrc + (i + WIN) * 1024); \
      sched_fence();                                                                    \
    }
    G2STEP(0, 9) G2STEP(1, 9) G2STEP(2, 9) G2STEP(3, 9) G2STEP(4, 9) G2STEP(5, 9) G2STEP(6, 9) G2STEP(7, 9) G2STEP(8, 9)
    G2STEP(9, 9) G2STEP(10, 9) G2STEP(11, 9) G2STEP(12, 9) G2STEP(13, 9) G2STEP(14, 9) G2STEP(15, 9) G2STEP(16, 9) G2STEP(17, 9)
    G2STEP(18, 9) G2STEP(19, 8) G2STEP(20, 7) G2STEP(21, 6) G2STEP(22, 5) G2STEP(23, 4) G2STEP(24, 3) G2STEP(25, 2) G2STEP(26, 1)
    G2STEP(27, 0)
    STAMP(8);
    f32x16 keep = kk ? acc1 : acc0, give = kk ? acc0 : acc1;
#pragma unroll
    for (int q = 0; q < 4; ++q)
      *(LAS f32x4*)(L_RED + w * 4096 + q * 1024 + lv16) = f32x4{give[4 * q], give[4 * q + 1], give[4 * q + 2], give[4 * q + 3]};
    wait_lgkm0();
    *(volatile LAS u32*)(L_FLG + w * 4) = 1;
    for (u32 spin = 0; spin < (SPIN_CAP << 4); ++spin)
      if (*(volatile LAS u32*)(L_FLG + (w ^ 2) * 4) != 0) break;
    asm volatile("" ::: "memory");
#pragma unroll
    for (int q = 0; q < 4; ++q) {
      f32x4 o = lds_f4(L_RED + (w ^ 2) * 4096 + q * 1024 + lv16);
#pragma unroll
      for (int e = 0; e < 4; ++e) keep[4 * q + e] += o[e];
    }
    STAMP(9);
    const u32 l0 = lane == 0 ? 0u : 0xfffffff0u - WS_BYTES;
    const u32 pslot = m * 4 + g * 2 + kk;
    if (hh == 0) {
      const u32 pdst = WS_P + (pk * 32 + pslot) * 4096;
#pragma unroll
      for (int q = 0; q < 4; ++q)
        st16_sc1(__builtin_bit_cast(v4u, f32x4{keep[4 * q], keep[4 * q + 1], keep[4 * q + 2], keep[4 * q + 3]}), rws, lv16, pdst + q * 1024);
      wait_vm<0>();
      st4_sc1(tag, rws, l0, flg + 1024 + pslot * 16);
      STAMP(10);
    } else {
      const u32 pflag = ctl + 512 + (1 - kh) * 1536 + 1024 + pslot * 16;
      u32 fv;
      for (u32 spin = 0; spin < SPIN_CAP; ++spin) {
        ld4_sc1(fv, rws, 0, pflag);
        wait_vm<0>(fv);
        if (__builtin_amdgcn_readfirstlane(fv) == tag) break;
      }
      STAMP(10);
      const u32 psrc = WS_P + ((pk ^ 1) * 32 + pslot) * 4096;
      v4u pr[4];
#pragma unroll
      for (int q = 0; q < 4; ++q) ld16_sc1(pr[q], rws, lv16, psrc + q * 1024);
      v4u bb0 = *(const LAS v4u*)(L_B2 + kk * 64 + h * 32), bb1 = *(const LAS v4u*)(L_B2 + kk * 64 + h * 32 + 16);
      asm volatile("s_waitcnt vmcnt(0)" : "+v"(pr[0]), "+v"(pr[1]), "+v"(pr[2]), "+v"(pr[3])::"memory");
      v4u o0, o1;
#pragma unroll
      for (int e = 0; e < 4; ++e) {
        o0[e] = pack2(keep[2 * e] + asf(pr[e >> 1][(2 * e) & 3]) + bf_lo(bb0[e]),
                      keep[2 * e + 1] + asf(pr[e >> 1][(2 * e + 1) & 3]) + bf_hi(bb0[e]));
        o1[e] = pack2(keep[8 + 2 * e] + asf(pr[2 + (e >> 1)][(2 * e) & 3]) + bf_lo(bb1[e]),
                      keep[8 + 2 * e + 1] + asf(pr[2 + (e >> 1)][(2 * e + 1) & 3]) + bf_hi(bb1[e]));
      }
      const v4u ro = mk_rsrc(out + panel * (128 * D * 2), 128 * D * 2);
      const u32 ov = (32 * g + t31) * 1024 + h * 32;
      const u32 os = rowO + m * 128 + kk * 64;
      st16(o0, ro, ov, os);
      st16(o1, ro, ov + 16, os);
      if (kh == 0 && m == 0 && w == 4) st4_sc1(tag, rws, l0, ctl);
    }
    STAMP(11);
  }
}

extern "C" void kernel_launch(void* const* d_in, const int* in_sizes, int n_in, void* d_out, int out_size, void* d_ws,
                              size_t ws_size, hipStream_t stream) {
  (void)in_sizes; (void)n_in; (void)out_size; (void)ws_size;
  (void)hipFuncSetAttribute((const void*)mixer_mlp, hipFuncAttributeMaxDynamicSharedMemorySize, L_BYTES);
  mixer_mlp<<<256, 512, L_BYTES, stream>>>((const char*)d_in[0], (const char*)d_in[1], (const char*)d_in[2], (const char*)d_in[3],
                                           (const char*)d_in[4], (char*)d_out, (char*)d_ws);
}
```

```cpp
#include <hip/hip_runtime.h>
#include <stdint.h>
#include <stdio.h>

typedef unsigned u32;
typedef unsigned v4u __attribute__((ext_vector_type(4)));
typedef unsigned v2u __attribute__((ext_vector_type(2)));
typedef short s16x4 __attribute__((ext_vector_type(4)));
typedef short s16x8 __attribute__((ext_vector_type(8)));
typedef __bf16 bf16x2 __attribute__((ext_vector_type(2)));
typedef __bf16 bf16x8 __attribute__((ext_vector_type(8)));
typedef float f32x4 __attribute__((ext_vector_type(4)));
typedef float f32x16 __attribute__((ext_vector_type(16)));
#define LAS __attribute__((address_space(3)))
#define DI __device__ __forceinline__

namespace {
constexpr int D = 512, F = 1792;
constexpr u32 WS_CTL = 0;
constexpr u32 WS_STAT = 65536;
constexpr u32 WS_H = 1u << 20;
constexpr u32 WS_P = 9u << 20;
constexpr u32 WS_BYTES = 14u << 20;
constexpr u32 L_SLOT = 24576;
constexpr u32 L_XO = 90112;
constexpr u32 L_RED = 114688;
constexpr u32 L_CS = 155648;
constexpr u32 L_B1 = L_CS + 1024;
constexpr u32 L_B2 = L_B1 + 256;
constexpr u32 L_FLG = L_B2 + 256;
constexpr u32 L_BYTES = 163840;
constexpr u32 SPIN_CAP = 1u << 18;

DI v4u mk_rsrc(const void* p, u32 bytes) {
  uint64_t a = (uint64_t)p;
  v4u r;
  r.x = (u32)a; r.y = (u32)(a >> 32) & 0xffffu; r.z = bytes; r.w = 0x00020000u;
  return r;
}
DI void dma16(v4u rs, u32 voff, u32 soff, u32 lds) {
  asm volatile("s_mov_b32 m0, %3\n\ts_nop 0\n\tbuffer_load_dwordx4 %0, %1, %2 offen lds" ::"v"(voff), "s"(rs), "s"(soff), "s"(lds) : "memory");
}
DI void dma4(v4u rs, u32 voff, u32 soff, u32 lds) {
  asm volatile("s_mov_b32 m0, %3\n\ts_nop 0\n\tbuffer_load_dword %0, %1, %2 offen lds" ::"v"(voff), "s"(rs), "s"(soff), "s"(lds) : "memory");
}
DI void ld16_sc1(v4u& d, v4u rs, u32 voff, u32 soff) {
  asm volatile("buffer_load_dwordx4 %0, %1, %2, %3 offen sc1" : "=v"(d) : "v"(voff), "s"(rs), "s"(soff) : "memory");
}
DI void ld16(v4u& d, v4u rs, u32 voff, u32 soff) {
  asm volatile("buffer_load_dwordx4 %0, %1, %2, %3 offen" : "=v"(d) : "v"(voff), "s"(rs), "s"(soff) : "memory");
}
DI void ld4_sc1(u32& d, v4u rs, u32 voff, u32 soff) {
  asm volatile("buffer_load_dword %0, %1, %2, %3 offen sc1" : "=v"(d) : "v"(voff), "s"(rs), "s"(soff) : "memory");
}
DI void st16_sc1(v4u d, v4u rs, u32 voff, u32 soff) {
  asm volatile("buffer_store_dwordx4 %0, %1, %2, %3 offen sc1\n\ts_nop 1" ::"v"(d), "v"(voff), "s"(rs), "s"(soff) : "memory");
}
DI void st16(v4u d, v4u rs, u32 voff, u32 soff) {
  asm volatile("buffer_store_dwordx4 %0, %1, %2, %3 offen\n\ts_nop 1" ::"v"(d), "v"(voff), "s"(rs), "s"(soff) : "memory");
}
DI void st4_sc1(u32 d, v4u rs, u32 voff, u32 soff) {
  asm volatile("buffer_store_dword %0, %1, %2, %3 offen sc1" ::"v"(d), "v"(voff), "s"(rs), "s"(soff) : "memory");
}
template <int N> DI void wait_vm() { asm volatile("s_waitcnt vmcnt(%0)" ::"i"(N) : "memory"); }
template <int N, class T> DI void wait_vm(T& pin) { asm volatile("s_waitcnt vmcnt(%1)" : "+v"(pin) : "i"(N) : "memory"); }
DI void wait_lgkm0() { __builtin_amdgcn_s_waitcnt(0xC07F); asm volatile("" ::: "memory"); }
DI void bar() { asm volatile("s_barrier" ::: "memory"); }
DI void sched_fence() { __builtin_amdgcn_sched_barrier(0); }

DI bf16x8 lds_b128(u32 a) { return *(const LAS bf16x8*)a; }
DI f32x4 lds_f4(u32 a) { return *(const LAS f32x4*)a; }
DI bf16x8 lds_tr(u32 a0, u32 a1) {
  s16x4 lo = __builtin_amdgcn_ds_read_tr16_b64_v4i16((LAS s16x4*)a0);
  s16x4 hi = __builtin_amdgcn_ds_read_tr16_b64_v4i16((LAS s16x4*)a1);
  return __builtin_bit_cast(bf16x8, __builtin_shufflevector(lo, hi, 0, 1, 2, 3, 4, 5, 6, 7));
}
DI f32x16 mfma32(bf16x8 a, bf16x8 b, f32x16 c) { return __builtin_amdgcn_mfma_f32_32x32x16_bf16(a, b, c, 0, 0, 0); }
DI f32x4 mfma16(bf16x8 a, bf16x8 b, f32x4 c) { return __builtin_amdgcn_mfma_f32_16x16x32_bf16(a, b, c, 0, 0, 0); }
DI u32 pack2(float a, float b) { bf16x2 v = {(__bf16)a, (__bf16)b}; return __builtin_bit_cast(u32, v); }
DI float asf(u32 w) { return __builtin_bit_cast(float, w); }
DI u32 asu(float f) { return __builtin_bit_cast(u32, f); }
DI bf16x2 asb2(u32 w) { return __builtin_bit_cast(bf16x2, w); }
DI float bf_lo(u32 w) { return __builtin_bit_cast(float, w << 16); }
DI float bf_hi(u32 w) { return __builtin_bit_cast(float, w & 0xffff0000u); }

DI float gelu1(float acc, float cs, float b1, float nm, float rs) {
  const float c1 = -2.3022081986f, c2 = -0.1029432397f;
  float t = __builtin_fmaf(nm, cs, acc);
  float v = __builtin_fmaf(t, rs, b1);
  float p = __builtin_fmaf(v * v, c2, c1);
  float e = __builtin_amdgcn_exp2f(v * p);
  return v * __builtin_amdgcn_rcpf(1.0f + e);
}
DI void gelu4(const f32x16& acc, int q, u32 csa, u32 b1a, float nm, float rs, v4u& o) {
  f32x4 c0 = lds_f4(csa + q * 32), c1 = lds_f4(csa + 512 + q * 32);
  v2u b = *(const LAS v2u*)(b1a + q * 16);
  float g0 = gelu1(acc[4 * q + 0], c0[0] + c1[0], bf_lo(b.x), nm, rs);
  float g1 = gelu1(acc[4 * q + 1], c0[1] + c1[1], bf_hi(b.x), nm, rs);
  float g2 = gelu1(acc[4 * q + 2], c0[2] + c1[2], bf_lo(b.y), nm, rs);
  float g3 = gelu1(acc[4 * q + 3], c0[3] + c1[3], bf_hi(b.y), nm, rs);
  o[2 * (q & 1)] = pack2(g0, g1); o[2 * (q & 1) + 1] = pack2(g2, g3);
}
#define STAMP(i)
}

__global__ __launch_bounds__(512, 2) void mixer_mlp(const char* x, const char* w1, const char* b1,
                                                    const char* w2, const char* b2, char* out,
                                                    char* ws) {
  extern __shared__ __attribute__((aligned(16))) char smem[];
  STAMP(0);
  asm volatile("" : "+s"(x), "+s"(w1), "+s"(b1), "+s"(w2), "+s"(b2), "+s"(out), "+s"(ws));
  const u32 lane = threadIdx.x & 63;
  const u32 w = __builtin_amdgcn_readfirstlane(threadIdx.x >> 6);
  const u32 xcd = blockIdx.x & 7, idx = blockIdx.x >> 3;
  const u32 kh = xcd & 1, panel = (xcd >> 1) * 4 + (idx & 3), m = idx >> 2;
  const u32 pk = panel * 2 + kh;
  const v4u rws = mk_rsrc(ws, WS_BYTES);
  const v4u rx = mk_rsrc(x + panel * (128 * D * 2), 128 * D * 2);
  const v4u rw2 = mk_rsrc(w2, F * D * 2);
  const u32 t31 = lane & 31, h = lane >> 5;
  const u32 rowF = (1 - kh) * 64 * 1024, rowO = kh * 64 * 1024;
  const u32 ctl = WS_CTL + panel * 4096;
  const u32 flg = ctl + 512 + kh * 1536;

  u32 ep;
  ld4_sc1(ep, rws, 0, ctl);
  if (lane < 8) *(LAS u32*)(L_FLG + lane * 4) = 0;
  if (w == 0) dma4(mk_rsrc(b2, D * 2), lane * 4, m * 128, L_B2);
  const u32 w2v = (8 * w + (lane >> 3)) * 1024 + (((lane & 7) ^ ((lane >> 2) & 4)) * 16);
  const u32 w2s = kh * 896 * 1024 + m * 128;
  auto dma_w2 = [&](int i) { dma16(rw2, w2v, w2s + i * 65536, i * 8192 + w * 1024); };
  const u32 xrow = 8 * w + (lane >> 3);
  u32 tag;

  if (m == 0) {
    v4u xr[2][8];
    const u32 xv = xrow * 1024 + (lane & 7) * 16;
#pragma unroll
    for (int s = 0; s < 2; ++s)
#pragma unroll
      for (int i = 0; i < 8; ++i) ld16(xr[s][i], rx, xv, (s ? rowO : rowF) + i * 128);
    wait_vm<16>(ep);
    tag = __builtin_amdgcn_readfirstlane(ep) + 1;
#pragma unroll
    for (int s = 0; s < 2; ++s) {
      float sum = 0.f, sq = 0.f;
      const bf16x2 ones = {(__bf16)1.0f, (__bf16)1.0f};
#pragma unroll
      for (int i = 0; i < 8; ++i) {
        if (s == 0) { switch (i) { case 0: wait_vm<15>(xr[s][i]); break; case 1: wait_vm<14>(xr[s][i]); break; case 2: wait_vm<13>(xr[s][i]); break; case 3: wait_vm<12>(xr[s][i]); break;
                                   case 4: wait_vm<11>(xr[s][i]); break; case 5: wait_vm<10>(xr[s][i]); break; case 6: wait_vm<9>(xr[s][i]); break; default: wait_vm<8>(xr[s][i]); } }
        else { switch (i) { case 0: wait_vm<8>(xr[s][i]); break; case 1: wait_vm<7>(xr[s][i]); break; case 2: wait_vm<6>(xr[s][i]); break; case 3: wait_vm<5>(xr[s][i]); break;
                            case 4: wait_vm<4>(xr[s][i]); break; case 5: wait_vm<3>(xr[s][i]); break; case 6: wait_vm<2>(xr[s][i]); break; default: wait_vm<1>(xr[s][i]); } }
#pragma unroll
        for (int c = 0; c < 4; ++c) {
          bf16x2 v = asb2(xr[s][i][c]);
          sum = __builtin_amdgcn_fdot2_f32_bf16(v, ones, sum, false);
          sq = __builtin_amdgcn_fdot2_f32_bf16(v, v, sq, false);
        }
      }
#pragma unroll
      for (int o = 1; o < 8; o <<= 1) { sum += __shfl_xor(sum, o); sq += __shfl_xor(sq, o); }
      const float mean = sum * (1.0f / D);
      const float var = fmaxf(sq * (1.0f / D) - mean * mean, 0.f);
      const float rs = __builtin_amdgcn_rsqf(var + 1e-5f);
      v4u g = {__builtin_bit_cast(u32, -mean), tag, __builtin_bit_cast(u32, rs), tag};
      st16_sc1(g, rws, (lane & 7) ? 0xfffffff0u - WS_BYTES : (s * 64 + xrow) * 16, WS_STAT + pk * 2048);
    }
    STAMP(1);
#pragma unroll
    for (int i = 0; i < 14; ++i) dma_w2(i);
    wait_vm<0>();
    STAMP(6);
    bar();
  } else {
    const u32 j = m - 1, g = w & 1, ft = w >> 1;
    const v4u rw1 = mk_rsrc(w1, D * F * 2);
    const u32 f0 = kh * 896 + j * 128;
    if (w == 1) dma4(mk_rsrc(b1, F * 2), lane * 4, f0 * 2, L_B1);
    const u32 xv = xrow * 1024 + (((lane & 7) ^ ((xrow >> 1) & 7)) * 16);
    const u32 w1v = (8 * w + (lane >> 4)) * (F * 2) + (((lane & 15) ^ ((lane >> 4) << 2)) * 16);
    const u32 w1s = f0 * 2;
    auto dma_g = [&](int kg) {
      const u32 sl = (kg % 3) * L_SLOT;
      dma16(rx, xv, rowF + kg * 128, sl + w * 1024);
      dma16(rw1, w1v, w1s + kg * 64 * (F * 2), sl + 8192 + w * 2048);
      dma16(rw1, w1v, w1s + (kg * 64 + 4) * (F * 2), sl + 8192 + w * 2048 + 1024);
    };
    auto dma_xo = [&](int kg) { dma16(rx, xv, rowO + kg * 128, L_XO + kg * 8192 + w * 1024); };
    dma_g(0);
    dma_g(1);
    u32 xa[4];
#pragma unroll
    for (int k = 0; k < 4; ++k) xa[k] = (32 * g + t31) * 128 + (((2 * (k ^ (2 * g)) + h) ^ ((t31 >> 1) & 7)) * 16);
    const u32 q4 = (lane >> 2) & 3;
    const u32 wa = 8192 + (8 * h + q4) * 256 + ((ft ^ q4) * 64) + ((lane >> 4) & 1) * 32 + (lane & 3) * 8;
    const u32 wa2[2] = {wa + g * 8192, wa + 8192 - g * 8192};
    const u32 selw = ((lane & 15) == ((lane >> 4) & 1)) ? 0x3f803f80u : 0u;
    const bf16x8 sel = __builtin_bit_cast(bf16x8, v4u{selw, selw, selw, selw});
    bf16x8 wf[32], xf[2][4];
    f32x16 accF = {}, accO = {};
    f32x4 cs = {};
    v4u st0, st1;
    const u32 stv = (32 * g + t31) * 16;
#pragma unroll
    for (int kg = 0; kg < 8; ++kg) {
      wait_vm<3>();
      bar();
      if (kg == 0) STAMP(1);
      if (kg == 7) ld16_sc1(st0, rws, stv, WS_STAT + pk * 2048);
      if (kg < 6) dma_g(kg + 2);
      else { dma_xo(3 * (kg - 6)); dma_xo(3 * (kg - 6) + 1); dma_xo(3 * (kg - 6) + 2); }
      const u32 sl = (kg % 3) * L_SLOT;
#pragma unroll
      for (int k = 0; k < 4; ++k) {
        xf[kg & 1][k] = lds_b128(xa[k] + sl);
        wf[4 * kg + k] = lds_tr(wa2[k >> 1] + sl + (k & 1) * 4096, wa2[k >> 1] + sl + (k & 1) * 4096 + 1024);
      }
      if (kg > 0) {
#pragma unroll
        for (int k = 0; k < 4; ++k) accF = mfma32(wf[4 * (kg - 1) + k], xf[(kg - 1) & 1][k], accF);
        cs = mfma16(sel, wf[4 * (kg - 1) + 0], cs); cs = mfma16(sel, wf[4 * (kg - 1) + 1], cs);
      }
      wait_lgkm0();
      sched_fence();
    }
    cs = mfma16(sel, wf[28], cs); cs = mfma16(sel, wf[29], cs);
#pragma unroll
    for (int k = 0; k < 4; ++k) accF = mfma32(wf[28 + k], xf[1][k], accF);
    if (lane < 16) {
      *(LAS float*)(L_CS + g * 512 + ft * 128 + lane * 4) = cs[0];
      *(LAS float*)(L_CS + g * 512 + ft * 128 + 64 + lane * 4) = cs[1];
    }
    wait_lgkm0();
    wait_vm<3>(st0);
    asm volatile("" : "+v"(ep));
    tag = __builtin_amdgcn_readfirstlane(ep) + 1;
    for (u32 spin = 0; __builtin_amdgcn_ballot_w64(st0.y != tag || st0.w != tag) != 0 && spin < SPIN_CAP; ++spin) {
      ld16_sc1(st0, rws, stv, WS_STAT + pk * 2048);
      wait_vm<0>(st0);
    }
    bar();
    STAMP(2);
    const u32 csa = L_CS + ft * 128 + h * 16, b1a = L_B1 + ft * 64 + h * 8;
    const u32 hbase = WS_H + pk * (4 * 56 * 1024) + g * (56 * 1024) + (8 * j + 2 * ft) * 1024;
    const u32 lv16 = lane * 16;
    const u32 fidx = 4 * j + ft, fkk = fidx >= 14 ? 1 : 0;
    const u32 myflag = flg + g * 256 + fkk * 128 + (fidx - 14 * fkk) * 4;
    const u32 l0 = lane == 0 ? 0u : 0xfffffff0u - WS_BYTES;
    v4u ho;
    float nm = asf(st0.x), rs = asf(st0.z);
#pragma unroll
    for (int k = 0; k < 4; ++k) xf[0][k] = lds_b128(xa[k] + L_XO);
#pragma unroll
    for (int s = 0; s < 8; ++s) {
      if (s == 2) { wait_vm<4>(); bar(); }
      if (s == 5) { wait_vm<10>(); bar(); }
      if (s == 0) { dma_xo(6); dma_xo(7); }
      else if (s == 1) { dma_w2(9); dma_w2(10); }
      else if (s < 5) { dma_w2(2 * s - 4); dma_w2(2 * s - 3); }
      else if (s == 5) { dma_w2(6); dma_w2(7); ld16_sc1(st1, rws, stv + 1024, WS_STAT + pk * 2048); }
      else if (s == 6) { dma_w2(8); dma_w2(11); }
      else { dma_w2(12); dma_w2(13); }
      if (s < 7) {
#pragma unroll
        for (int k = 0; k < 4; ++k) xf[(s + 1) & 1][k] = lds_b128(xa[k] + L_XO + (s + 1) * 8192);
      }
#pragma unroll
      for (int k = 0; k < 4; ++k) accO = mfma32(wf[4 * s + k], xf[s & 1][k], accO);
      if (s == 0) gelu4(accF, 0, csa, b1a, nm, rs, ho);
      if (s == 2) { gelu4(accF, 1, csa, b1a, nm, rs, ho); st16_sc1(ho, rws, lv16, hbase); }
      if (s == 3) gelu4(accF, 2, csa, b1a, nm, rs, ho);
      if (s == 4) { gelu4(accF, 3, csa, b1a, nm, rs, ho); st16_sc1(ho, rws, lv16, hbase + 1024); }
      wait_lgkm0();
      sched_fence();
    }
    STAMP(3);
    wait_vm<7>();
    st4_sc1(tag, rws, l0, myflag);
    wait_vm<5>(st1);
    for (u32 spin = 0; __builtin_amdgcn_ballot_w64(st1.y != tag || st1.w != tag) != 0 && spin < SPIN_CAP; ++spin) {
      ld16_sc1(st1, rws, stv + 1024, WS_STAT + pk * 2048);
      wait_vm<0>(st1);
    }
    STAMP(4);
    nm = asf(st1.x); rs = asf(st1.z);
    gelu4(accO, 0, csa, b1a, nm, rs, ho);
    gelu4(accO, 1, csa, b1a, nm, rs, ho);
    st16_sc1(ho, rws, lv16, hbase + 2 * 56 * 1024);
    gelu4(accO, 2, csa, b1a, nm, rs, ho);
    gelu4(accO, 3, csa, b1a, nm, rs, ho);
    st16_sc1(ho, rws, lv16, hbase + 2 * 56 * 1024 + 1024);
    STAMP(5);
    wait_vm<3>();
    bar();
    wait_vm<0>();
    st4_sc1(tag, rws, l0, myflag + 512);
    STAMP(6);
  }

  {
    const u32 hh = w >> 2, g = w & 1, kk = (w >> 1) & 1;
    const u32 lv16 = lane * 16;
    {
      const u32 pv = (lane < 14 ? lane : 0) * 4;
      u32 fv;
      for (u32 spin = 0; spin < SPIN_CAP; ++spin) {
        ld4_sc1(fv, rws, pv, flg + hh * 512 + g * 256 + kk * 128);
        wait_vm<0>(fv);
        if (__builtin_amdgcn_ballot_w64(fv != tag) == 0) break;
      }
    }
    STAMP(7);
    const u32 hsrc = WS_H + pk * (4 * 56 * 1024) + (hh * 2 + g) * (56 * 1024) + kk * (28 * 1024);
    constexpr int WIN = 10;
    v4u hf[28];
#pragma unroll
    for (int i = 0; i < WIN; ++i) ld16_sc1(hf[i], rws, lv16, hsrc + i * 1024);
    const u32 q4 = (lane >> 2) & 3, p4 = lane & 3, blk = (lane >> 4) & 1;
    const u32 a0 = kk * 57344 + (4 * h + q4) * 128 + ((q4 >> 1) * 64) + (4 * (p4 & 1) + 2 * blk + (p4 >> 1)) * 8;
    const u32 a1 = a0 ^ 64;
    f32x16 acc0 = {}, acc1 = {};
    bf16x8 wa0[2], wa1[2];
    wa0[0] = lds_tr(a0, a0 + 1024);
    wa1[0] = lds_tr(a1, a1 + 1024);
#define G2STEP(i, N)                                                                   \
    {                                                                                   \
      if (i + 1 < 28) {                                                                 \
        wa0[(i + 1) & 1] = lds_tr(a0 + (i + 1) * 2048, a0 + (i + 1) * 2048 + 1024);     \
        wa1[(i + 1) & 1] = lds_tr(a1 + (i + 1) * 2048, a1 + (i + 1) * 2048 + 1024);     \
      }                                                                                 \
      wait_vm<N>(hf[i]);                                                                \
      acc0 = mfma32(wa0[i & 1], __builtin_bit_cast(bf16x8, hf[i]), acc0);               \
      acc1 = mfma32(wa1[i & 1], __builtin_bit_cast(bf16x8, hf[i]), acc1);               \
      if (i + WIN < 28) ld16_sc1(hf[(i + WIN) % 28], rws, lv16, hsrc + (i + WIN) * 1024); \
      sched_fence();                                                                    \
    }
    G2STEP(0, 9) G2STEP(1, 9) G2STEP(2, 9) G2STEP(3, 9) G2STEP(4, 9) G2STEP(5, 9) G2STEP(6, 9) G2STEP(7, 9) G2STEP(8, 9)
    G2STEP(9, 9) G2STEP(10, 9) G2STEP(11, 9) G2STEP(12, 9) G2STEP(13, 9) G2STEP(14, 9) G2STEP(15, 9) G2STEP(16, 9) G2STEP(17, 9)
    G2STEP(18, 9) G2STEP(19, 8) G2STEP(20, 7) G2STEP(21, 6) G2STEP(22, 5) G2STEP(23, 4) G2STEP(24, 3) G2STEP(25, 2) G2STEP(26, 1)
    G2STEP(27, 0)
    STAMP(8);
    f32x16 keep = kk ? acc1 : acc0, give = kk ? acc0 : acc1;
#pragma unroll
    for (int q = 0; q < 4; ++q)
      *(LAS f32x4*)(L_RED + w * 4096 + q * 1024 + lv16) = f32x4{give[4 * q], give[4 * q + 1], give[4 * q + 2], give[4 * q + 3]};
    wait_lgkm0();
    *(volatile LAS u32*)(L_FLG + w * 4) = 1;
    for (u32 spin = 0; spin < (SPIN_CAP << 4); ++spin)
      if (*(volatile LAS u32*)(L_FLG + (w ^ 2) * 4) != 0) break;
    asm volatile("" ::: "memory");
#pragma unroll
    for (int q = 0; q < 4; ++q) {
      f32x4 o = lds_f4(L_RED + (w ^ 2) * 4096 + q * 1024 + lv16);
#pragma unroll
      for (int e = 0; e < 4; ++e) keep[4 * q + e] += o[e];
    }
    STAMP(9);
    const u32 l0 = lane == 0 ? 0u : 0xfffffff0u - WS_BYTES;
    const u32 pslot = m * 4 + g * 2 + kk;
    if (hh == 0) {
      const u32 pdst = WS_P + (pk * 32 + pslot) * 4096;
#pragma unroll
      for (int q = 0; q < 4; ++q)
        st16_sc1(__builtin_bit_cast(v4u, f32x4{keep[4 * q], keep[4 * q + 1], keep[4 * q + 2], keep[4 * q + 3]}), rws, lv16, pdst + q * 1024);
      wait_vm<0>();
      st4_sc1(tag, rws, l0, flg + 1024 + pslot * 16);
      STAMP(10);
    } else {
      const u32 pflag = ctl + 512 + (1 - kh) * 1536 + 1024 + pslot * 16;
      u32 fv;
      for (u32 spin = 0; spin < SPIN_CAP; ++spin) {
        ld4_sc1(fv, rws, 0, pflag);
        wait_vm<0>(fv);
        if (__builtin_amdgcn_readfirstlane(fv) == tag) break;
      }
      STAMP(10);
      const u32 psrc = WS_P + ((pk ^ 1) * 32 + pslot) * 4096;
      v4u pr[4];
#pragma unroll
      for (int q = 0; q < 4; ++q) ld16_sc1(pr[q], rws, lv16, psrc + q * 1024);
      v4u bb0 = *(const LAS v4u*)(L_B2 + kk * 64 + h * 32), bb1 = *(const LAS v4u*)(L_B2 + kk * 64 + h * 32 + 16);
      asm volatile("s_waitcnt vmcnt(0)" : "+v"(pr[0]), "+v"(pr[1]), "+v"(pr[2]), "+v"(pr[3])::"memory");
      v4u o0, o1;
#pragma unroll
      for (int e = 0; e < 4; ++e) {
        o0[e] = pack2(keep[2 * e] + asf(pr[e >> 1][(2 * e) & 3]) + bf_lo(bb0[e]),
                      keep[2 * e + 1] + asf(pr[e >> 1][(2 * e + 1) & 3]) + bf_hi(bb0[e]));
        o1[e] = pack2(keep[8 + 2 * e] + asf(pr[2 + (e >> 1)][(2 * e) & 3]) + bf_lo(bb1[e]),
                      keep[8 + 2 * e + 1] + asf(pr[2 + (e >> 1)][(2 * e + 1) & 3]) + bf_hi(bb1[e]));
      }
      const v4u ro = mk_rsrc(out + panel * (128 * D * 2), 128 * D * 2);
      const u32 ov = (32 * g + t31) * 1024 + h * 32;
      const u32 os = rowO + m * 128 + kk * 64;
      st16(o0, ro, ov, os);
      st16(o1, ro, ov + 16, os);
      if (kh == 0 && m == 0 && w == 4) st4_sc1(tag, rws, l0, ctl);
    }
    STAMP(11);
  }
}

__global__ void dly(const char* x, const char* w1, const char* w2, const char* out, const char* ws) {
  unsigned long long t0 = __builtin_amdgcn_s_memrealtime();
  unsigned long long d = 1000ull * __builtin_ctzll((unsigned long long)out | (1ull << 30));
  while (__builtin_amdgcn_s_memrealtime() - t0 < d) __builtin_amdgcn_s_sleep(1);
}
extern "C" void kernel_launch(void* const* d_in, const int* in_sizes, int n_in, void* d_out, int out_size, void* d_ws,
                              size_t ws_size, hipStream_t stream) {
  (void)in_sizes; (void)n_in; (void)out_size; (void)ws_size;
  (void)hipFuncSetAttribute((const void*)mixer_mlp, hipFuncAttributeMaxDynamicSharedMemorySize, L_BYTES);
  fprintf(stderr, "ptrs %p %p %p %p %p out %p ws %p\n", d_in[0], d_in[1], d_in[2], d_in[3], d_in[4], d_out, d_ws);
  dly<<<1, 64, 0, stream>>>((const char*)d_in[0], (const char*)d_in[1], (const char*)d_in[3], (const char*)d_out, (const char*)d_ws);
  mixer_mlp<<<256, 512, L_BYTES, stream>>>((const char*)d_in[0], (const char*)d_in[1], (const char*)d_in[2], (const char*)d_in[3],
                                           (const char*)d_in[4], (char*)d_out, (char*)d_ws);
}
```

```cpp
#define STAMPS
#include <hip/hip_runtime.h>
#include <stdint.h>

typedef unsigned u32;
typedef unsigned v4u __attribute__((ext_vector_type(4)));
typedef unsigned v2u __attribute__((ext_vector_type(2)));
typedef short s16x4 __attribute__((ext_vector_type(4)));
typedef short s16x8 __attribute__((ext_vector_type(8)));
typedef __bf16 bf16x2 __attribute__((ext_vector_type(2)));
typedef __bf16 bf16x8 __attribute__((ext_vector_type(8)));
typedef float f32x4 __attribute__((ext_vector_type(4)));
typedef float f32x16 __attribute__((ext_vector_type(16)));
#define LAS __attribute__((address_space(3)))
#define DI __device__ __forceinline__

namespace {
constexpr int D = 512, F = 1792;
constexpr u32 WS_CTL = 0;
constexpr u32 WS_STAT = 65536;
constexpr u32 WS_H = 1u << 20;
constexpr u32 WS_P = 9u << 20;
constexpr u32 WS_BYTES = 14u << 20;
constexpr u32 L_SLOT = 24576;
constexpr u32 L_XO = 90112;
constexpr u32 L_RED = 114688;
constexpr u32 L_CS = 155648;
constexpr u32 L_B1 = L_CS + 1024;
constexpr u32 L_B2 = L_B1 + 256;
constexpr u32 L_FLG = L_B2 + 256;
constexpr u32 L_BYTES = 163840;
constexpr u32 SPIN_CAP = 1u << 18;

DI v4u mk_rsrc(const void* p, u32 bytes) {
  uint64_t a = (uint64_t)p;
  v4u r;
  r.x = (u32)a; r.y = (u32)(a >> 32) & 0xffffu; r.z = bytes; r.w = 0x00020000u;
  return r;
}
DI void dma16(v4u rs, u32 voff, u32 soff, u32 lds) {
  asm volatile("s_mov_b32 m0, %3\n\ts_nop 0\n\tbuffer_load_dwordx4 %0, %1, %2 offen lds" ::"v"(voff), "s"(rs), "s"(soff), "s"(lds) : "memory");
}
DI void dma4(v4u rs, u32 voff, u32 soff, u32 lds) {
  asm volatile("s_mov_b32 m0, %3\n\ts_nop 0\n\tbuffer_load_dword %0, %1, %2 offen lds" ::"v"(voff), "s"(rs), "s"(soff), "s"(lds) : "memory");
}
DI void ld16_sc1(v4u& d, v4u rs, u32 voff, u32 soff) {
  asm volatile("buffer_load_dwordx4 %0, %1, %2, %3 offen sc1" : "=v"(d) : "v"(voff), "s"(rs), "s"(soff) : "memory");
}
DI void ld16(v4u& d, v4u rs, u32 voff, u32 soff) {
  asm volatile("buffer_load_dwordx4 %0, %1, %2, %3 offen" : "=v"(d) : "v"(voff), "s"(rs), "s"(soff) : "memory");
}
DI void ld4_sc1(u32& d, v4u rs, u32 voff, u32 soff) {
  asm volatile("buffer_load_dword %0, %1, %2, %3 offen sc1" : "=v"(d) : "v"(voff), "s"(rs), "s"(soff) : "memory");
}
DI void st16_sc1(v4u d, v4u rs, u32 voff, u32 soff) {
  asm volatile("buffer_store_dwordx4 %0, %1, %2, %3 offen sc1\n\ts_nop 1" ::"v"(d), "v"(voff), "s"(rs), "s"(soff) : "memory");
}
DI void st16(v4u d, v4u rs, u32 voff, u32 soff) {
  asm volatile("buffer_store_dwordx4 %0, %1, %2, %3 offen sc0 sc1\n\ts_nop 1" ::"v"(d), "v"(voff), "s"(rs), "s"(soff) : "memory");
}
DI void st4_sc1(u32 d, v4u rs, u32 voff, u32 soff) {
  asm volatile("buffer_store_dword %0, %1, %2, %3 offen sc1" ::"v"(d), "v"(voff), "s"(rs), "s"(soff) : "memory");
}
template <int N> DI void wait_vm() { asm volatile("s_waitcnt vmcnt(%0)" ::"i"(N) : "memory"); }
template <int N, class T> DI void wait_vm(T& pin) { asm volatile("s_waitcnt vmcnt(%1)" : "+v"(pin) : "i"(N) : "memory"); }
DI void wait_lgkm0() { __builtin_amdgcn_s_waitcnt(0xC07F); asm volatile("" ::: "memory"); }
DI void bar() { asm volatile("s_barrier" ::: "memory"); }
DI void sched_fence() { __builtin_amdgcn_sched_barrier(0); }

DI bf16x8 lds_b128(u32 a) { return *(const LAS bf16x8*)a; }
DI f32x4 lds_f4(u32 a) { return *(const LAS f32x4*)a; }
DI bf16x8 lds_tr(u32 a0, u32 a1) {
  s16x4 lo = __builtin_amdgcn_ds_read_tr16_b64_v4i16((LAS s16x4*)a0);
  s16x4 hi = __builtin_amdgcn_ds_read_tr16_b64_v4i16((LAS s16x4*)a1);
  return __builtin_bit_cast(bf16x8, __builtin_shufflevector(lo, hi, 0, 1, 2, 3, 4, 5, 6, 7));
}
DI f32x16 mfma32(bf16x8 a, bf16x8 b, f32x16 c) { return __builtin_amdgcn_mfma_f32_32x32x16_bf16(a, b, c, 0, 0, 0); }
DI f32x4 mfma16(bf16x8 a, bf16x8 b, f32x4 c) { return __builtin_amdgcn_mfma_f32_16x16x32_bf16(a, b, c, 0, 0, 0); }
DI u32 pack2(float a, float b) { bf16x2 v = {(__bf16)a, (__bf16)b}; return __builtin_bit_cast(u32, v); }
DI float asf(u32 w) { return __builtin_bit_cast(float, w); }
DI u32 asu(float f) { return __builtin_bit_cast(u32, f); }
DI bf16x2 asb2(u32 w) { return __builtin_bit_cast(bf16x2, w); }
DI float bf_lo(u32 w) { return __builtin_bit_cast(float, w << 16); }
DI float bf_hi(u32 w) { return __builtin_bit_cast(float, w & 0xffff0000u); }

DI float gelu1(float acc, float cs, float b1, float nm, float rs) {
  const float c1 = -2.3022081986f, c2 = -0.1029432397f;
  float t = __builtin_fmaf(nm, cs, acc);
  float v = __builtin_fmaf(t, rs, b1);
  float p = __builtin_fmaf(v * v, c2, c1);
  float e = __builtin_amdgcn_exp2f(v * p);
  return v * __builtin_amdgcn_rcpf(1.0f + e);
}
DI void gelu4(const f32x16& acc, int q, u32 csa, u32 b1a, float nm, float rs, v4u& o) {
  f32x4 c0 = lds_f4(csa + q * 32), c1 = lds_f4(csa + 512 + q * 32);
  v2u b = *(const LAS v2u*)(b1a + q * 16);
  float g0 = gelu1(acc[4 * q + 0], c0[0] + c1[0], bf_lo(b.x), nm, rs);
  float g1 = gelu1(acc[4 * q + 1], c0[1] + c1[1], bf_hi(b.x), nm, rs);
  float g2 = gelu1(acc[4 * q + 2], c0[2] + c1[2], bf_lo(b.y), nm, rs);
  float g3 = gelu1(acc[4 * q + 3], c0[3] + c1[3], bf_hi(b.y), nm, rs);
  o[2 * (q & 1)] = pack2(g0, g1); o[2 * (q & 1) + 1] = pack2(g2, g3);
}
#define STAMP(i) do { unsigned long long t_; asm volatile("s_memrealtime %0\n\ts_waitcnt lgkmcnt(0)" : "=s"(t_) :: "memory"); stamps[i] = t_; } while (0)
}

__global__ __launch_bounds__(512, 2) void mixer_mlp(const char* x, const char* w1, const char* b1,
                                                    const char* w2, const char* b2, char* out,
                                                    char* ws) {
  extern __shared__ __attribute__((aligned(16))) char smem[];
  unsigned long long stamps[16];
  for (int i = 0; i < 16; ++i) stamps[i] = 0;
  STAMP(0);
  unsigned long long c0_ = __builtin_amdgcn_s_memtime();
  asm volatile("" : "+s"(x), "+s"(w1), "+s"(b1), "+s"(w2), "+s"(b2), "+s"(out), "+s"(ws));
  const u32 lane = threadIdx.x & 63;
  const u32 w = __builtin_amdgcn_readfirstlane(threadIdx.x >> 6);
  const u32 xcd = blockIdx.x & 7, idx = blockIdx.x >> 3;
  const u32 kh = xcd & 1, panel = (xcd >> 1) * 4 + (idx & 3), m = idx >> 2;
  const u32 pk = panel * 2 + kh;
  const v4u rws = mk_rsrc(ws, WS_BYTES);
  const v4u rx = mk_rsrc(x + panel * (128 * D * 2), 128 * D * 2);
  const v4u rw2 = mk_rsrc(w2, F * D * 2);
  const u32 t31 = lane & 31, h = lane >> 5;
  const u32 rowF = (1 - kh) * 64 * 1024, rowO = kh * 64 * 1024;
  const u32 ctl = WS_CTL + panel * 4096;
  const u32 flg = ctl + 512 + kh * 1536;

  u32 ep;
  ld4_sc1(ep, rws, 0, ctl);
  if (lane < 8) *(LAS u32*)(L_FLG + lane * 4) = 0;
  if (w == 0) dma4(mk_rsrc(b2, D * 2), lane * 4, m * 128, L_B2);
  const u32 w2v = (8 * w + (lane >> 3)) * 1024 + (((lane & 7) ^ ((lane >> 2) & 4)) * 16);
  const u32 w2s = kh * 896 * 1024 + m * 128;
  auto dma_w2 = [&](int i) { dma16(rw2, w2v, w2s + i * 65536, i * 8192 + w * 1024); };
  const u32 xrow = 8 * w + (lane >> 3);
  u32 tag;

  if (m == 0) {
    v4u xr[2][8];
    const u32 xv = xrow * 1024 + (lane & 7) * 16;
#pragma unroll
    for (int s = 0; s < 2; ++s)
#pragma unroll
      for (int i = 0; i < 8; ++i) ld16(xr[s][i], rx, xv, (s ? rowO : rowF) + i * 128);
    wait_vm<16>(ep);
    tag = __builtin_amdgcn_readfirstlane(ep) + 1;
#pragma unroll
    for (int s = 0; s < 2; ++s) {
      float sum = 0.f, sq = 0.f;
      const bf16x2 ones = {(__bf16)1.0f, (__bf16)1.0f};
#pragma unroll
      for (int i = 0; i < 8; ++i) {
        if (s == 0) { switch (i) { case 0: wait_vm<15>(xr[s][i]); break; case 1: wait_vm<14>(xr[s][i]); break; case 2: wait_vm<13>(xr[s][i]); break; case 3: wait_vm<12>(xr[s][i]); break;
                                   case 4: wait_vm<11>(xr[s][i]); break; case 5: wait_vm<10>(xr[s][i]); break; case 6: wait_vm<9>(xr[s][i]); break; default: wait_vm<8>(xr[s][i]); } }
        else { switch (i) { case 0: wait_vm<8>(xr[s][i]); break; case 1: wait_vm<7>(xr[s][i]); break; case 2: wait_vm<6>(xr[s][i]); break; case 3: wait_vm<5>(xr[s][i]); break;
                            case 4: wait_vm<4>(xr[s][i]); break; case 5: wait_vm<3>(xr[s][i]); break; case 6: wait_vm<2>(xr[s][i]); break; default: wait_vm<1>(xr[s][i]); } }
#pragma unroll
        for (int c = 0; c < 4; ++c) {
          bf16x2 v = asb2(xr[s][i][c]);
          sum = __builtin_amdgcn_fdot2_f32_bf16(v, ones, sum, false);
          sq = __builtin_amdgcn_fdot2_f32_bf16(v, v, sq, false);
        }
      }
#pragma unroll
      for (int o = 1; o < 8; o <<= 1) { sum += __shfl_xor(sum, o); sq += __shfl_xor(sq, o); }
      const float mean = sum * (1.0f / D);
      const float var = fmaxf(sq * (1.0f / D) - mean * mean, 0.f);
      const float rs = __builtin_amdgcn_rsqf(var + 1e-5f);
      v4u g = {__builtin_bit_cast(u32, -mean), tag, __builtin_bit_cast(u32, rs), tag};
      st16_sc1(g, rws, (lane & 7) ? 0xfffffff0u - WS_BYTES : (s * 64 + xrow) * 16, WS_STAT + pk * 2048);
    }
    STAMP(1);
#pragma unroll
    for (int i = 0; i < 14; ++i) dma_w2(i);
    wait_vm<0>();
    STAMP(6);
    bar();
  } else {
    const u32 j = m - 1, g = w & 1, ft = w >> 1;
    const v4u rw1 = mk_rsrc(w1, D * F * 2);
    const u32 f0 = kh * 896 + j * 128;
    if (w == 1) dma4(mk_rsrc(b1, F * 2), lane * 4, f0 * 2, L_B1);
    const u32 xv = xrow * 1024 + (((lane & 7) ^ ((xrow >> 1) & 7)) * 16);
    const u32 w1v = (8 * w + (lane >> 4)) * (F * 2) + (((lane & 15) ^ ((lane >> 4) << 2)) * 16);
    const u32 w1s = f0 * 2;
    auto dma_g = [&](int kg) {
      const u32 sl = (kg % 3) * L_SLOT;
      dma16(rx, xv, rowF + kg * 128, sl + w * 1024);
      dma16(rw1, w1v, w1s + kg * 64 * (F * 2), sl + 8192 + w * 2048);
      dma16(rw1, w1v, w1s + (kg * 64 + 4) * (F * 2), sl + 8192 + w * 2048 + 1024);
    };
    auto dma_xo = [&](int kg) { dma16(rx, xv, rowO + kg * 128, L_XO + kg * 8192 + w * 1024); };
    dma_g(0);
    dma_g(1);
    u32 xa[4];
#pragma unroll
    for (int k = 0; k < 4; ++k) xa[k] = (32 * g + t31) * 128 + (((2 * (k ^ (2 * g)) + h) ^ ((t31 >> 1) & 7)) * 16);
    const u32 q4 = (lane >> 2) & 3;
    const u32 wa = 8192 + (8 * h + q4) * 256 + ((ft ^ q4) * 64) + ((lane >> 4) & 1) * 32 + (lane & 3) * 8;
    const u32 wa2[2] = {wa + g * 8192, wa + 8192 - g * 8192};
    const u32 selw = ((lane & 15) == ((lane >> 4) & 1)) ? 0x3f803f80u : 0u;
    const bf16x8 sel = __builtin_bit_cast(bf16x8, v4u{selw, selw, selw, selw});
    bf16x8 wf[32], xf[2][4];
    f32x16 accF = {}, accO = {};
    f32x4 cs = {};
    v4u st0, st1;
    const u32 stv = (32 * g + t31) * 16;
#pragma unroll
    for (int kg = 0; kg < 8; ++kg) {
      wait_vm<3>();
      bar();
      if (kg == 0) STAMP(1);
      if (kg == 7) ld16_sc1(st0, rws, stv, WS_STAT + pk * 2048);
      if (kg < 6) dma_g(kg + 2);
      else { dma_xo(3 * (kg - 6)); dma_xo(3 * (kg - 6) + 1); dma_xo(3 * (kg - 6) + 2); }
      const u32 sl = (kg % 3) * L_SLOT;
#pragma unroll
      for (int k = 0; k < 4; ++k) {
        xf[kg & 1][k] = lds_b128(xa[k] + sl);
        wf[4 * kg + k] = lds_tr(wa2[k >> 1] + sl + (k & 1) * 4096, wa2[k >> 1] + sl + (k & 1) * 4096 + 1024);
      }
      if (kg > 0) {
#pragma unroll
        for (int k = 0; k < 4; ++k) accF = mfma32(wf[4 * (kg - 1) + k], xf[(kg - 1) & 1][k], accF);
        cs = mfma16(sel, wf[4 * (kg - 1) + 0], cs); cs = mfma16(sel, wf[4 * (kg - 1) + 1], cs);
      }
      wait_lgkm0();
      sched_fence();
    }
    cs = mfma16(sel, wf[28], cs); cs = mfma16(sel, wf[29], cs);
#pragma unroll
    for (int k = 0; k < 4; ++k) accF = mfma32(wf[28 + k], xf[1][k], accF);
    if (lane < 16) {
      *(LAS float*)(L_CS + g * 512 + ft * 128 + lane * 4) = cs[0];
      *(LAS float*)(L_CS + g * 512 + ft * 128 + 64 + lane * 4) = cs[1];
    }
    wait_lgkm0();
    wait_vm<3>(st0);
    asm volatile("" : "+v"(ep));
    tag = __builtin_amdgcn_readfirstlane(ep) + 1;
    for (u32 spin = 0; __builtin_amdgcn_ballot_w64(st0.y != tag || st0.w != tag) != 0 && spin < SPIN_CAP; ++spin) {
      ld16_sc1(st0, rws, stv, WS_STAT + pk * 2048);
      wait_vm<0>(st0);
    }
    bar();
    STAMP(2);
    const u32 csa = L_CS + ft * 128 + h * 16, b1a = L_B1 + ft * 64 + h * 8;
    const u32 hbase = WS_H + pk * (4 * 56 * 1024) + g * (56 * 1024) + (8 * j + 2 * ft) * 1024;
    const u32 lv16 = lane * 16;
    const u32 fidx = 4 * j + ft, fkk = fidx >= 14 ? 1 : 0;
    const u32 myflag = flg + g * 256 + fkk * 128 + (fidx - 14 * fkk) * 4;
    const u32 l0 = lane == 0 ? 0u : 0xfffffff0u - WS_BYTES;
    v4u ho;
    float nm = asf(st0.x), rs = asf(st0.z);
#pragma unroll
    for (int k = 0; k < 4; ++k) xf[0][k] = lds_b128(xa[k] + L_XO);
#pragma unroll
    for (int s = 0; s < 8; ++s) {
      if (s == 2) { wait_vm<4>(); bar(); }
      if (s == 5) { wait_vm<10>(); bar(); }
      if (s == 0) { dma_xo(6); dma_xo(7); }
      else if (s == 1) { dma_w2(9); dma_w2(10); }
      else if (s < 5) { dma_w2(2 * s - 4); dma_w2(2 * s - 3); }
      else if (s == 5) { dma_w2(6); dma_w2(7); ld16_sc1(st1, rws, stv + 1024, WS_STAT + pk * 2048); }
      else if (s == 6) { dma_w2(8); dma_w2(11); }
      else { dma_w2(12); dma_w2(13); }
      if (s < 7) {
#pragma unroll
        for (int k = 0; k < 4; ++k) xf[(s + 1) & 1][k] = lds_b128(xa[k] + L_XO + (s + 1) * 8192);
      }
#pragma unroll
      for (int k = 0; k < 4; ++k) accO = mfma32(wf[4 * s + k], xf[s & 1][k], accO);
      if (s == 0) gelu4(accF, 0, csa, b1a, nm, rs, ho);
      if (s == 2) { gelu4(accF, 1, csa, b1a, nm, rs, ho); st16_sc1(ho, rws, lv16, hbase); }
      if (s == 3) gelu4(accF, 2, csa, b1a, nm, rs, ho);
      if (s == 4) { gelu4(accF, 3, csa, b1a, nm, rs, ho); st16_sc1(ho, rws, lv16, hbase + 1024); }
      wait_lgkm0();
      sched_fence();
    }
    STAMP(3);
    wait_vm<7>();
    st4_sc1(tag, rws, l0, myflag);
    wait_vm<5>(st1);
    for (u32 spin = 0; __builtin_amdgcn_ballot_w64(st1.y != tag || st1.w != tag) != 0 && spin < SPIN_CAP; ++spin) {
      ld16_sc1(st1, rws, stv + 1024, WS_STAT + pk * 2048);
      wait_vm<0>(st1);
    }
    STAMP(4);
    nm = asf(st1.x); rs = asf(st1.z);
    gelu4(accO, 0, csa, b1a, nm, rs, ho);
    gelu4(accO, 1, csa, b1a, nm, rs, ho);
    st16_sc1(ho, rws, lv16, hbase + 2 * 56 * 1024);
    gelu4(accO, 2, csa, b1a, nm, rs, ho);
    gelu4(accO, 3, csa, b1a, nm, rs, ho);
    st16_sc1(ho, rws, lv16, hbase + 2 * 56 * 1024 + 1024);
    STAMP(5);
    wait_vm<3>();
    bar();
    wait_vm<0>();
    st4_sc1(tag, rws, l0, myflag + 512);
    STAMP(6);
  }

  {
    const u32 hh = w >> 2, g = w & 1, kk = (w >> 1) & 1;
    const u32 lv16 = lane * 16;
    {
      const u32 pv = (lane < 14 ? lane : 0) * 4;
      u32 fv;
      for (u32 spin = 0; spin < SPIN_CAP; ++spin) {
        ld4_sc1(fv, rws, pv, flg + hh * 512 + g * 256 + kk * 128);
        wait_vm<0>(fv);
        if (__builtin_amdgcn_ballot_w64(fv != tag) == 0) break;
      }
    }
    STAMP(7);
    const u32 hsrc = WS_H + pk * (4 * 56 * 1024) + (hh * 2 + g) * (56 * 1024) + kk * (28 * 1024);
    constexpr int WIN = 10;
    v4u hf[28];
#pragma unroll
    for (int i = 0; i < WIN; ++i) ld16_sc1(hf[i], rws, lv16, hsrc + i * 1024);
    const u32 q4 = (lane >> 2) & 3, p4 = lane & 3, blk = (lane >> 4) & 1;
    const u32 a0 = kk * 57344 + (4 * h + q4) * 128 + ((q4 >> 1) * 64) + (4 * (p4 & 1) + 2 * blk + (p4 >> 1)) * 8;
    const u32 a1 = a0 ^ 64;
    f32x16 acc0 = {}, acc1 = {};
    bf16x8 wa0[2], wa1[2];
    wa0[0] = lds_tr(a0, a0 + 1024);
    wa1[0] = lds_tr(a1, a1 + 1024);
#define G2STEP(i, N)                                                                   \
    {                                                                                   \
      if (i + 1 < 28) {                                                                 \
        wa0[(i + 1) & 1] = lds_tr(a0 + (i + 1) * 2048, a0 + (i + 1) * 2048 + 1024);     \
        wa1[(i + 1) & 1] = lds_tr(a1 + (i + 1) * 2048, a1 + (i + 1) * 2048 + 1024);     \
      }                                                                                 \
      wait_vm<N>(hf[i]);                                                                \
      acc0 = mfma32(wa0[i & 1], __builtin_bit_cast(bf16x8, hf[i]), acc0);               \
      acc1 = mfma32(wa1[i & 1], __builtin_bit_cast(bf16x8, hf[i]), acc1);               \
      if (i + WIN < 28) ld16_sc1(hf[(i + WIN) % 28], rws, lv16, hsrc + (i + WIN) * 1024); \
      sched_fence();                                                                    \
    }
    G2STEP(0, 9) G2STEP(1, 9) G2STEP(2, 9) G2STEP(3, 9) G2STEP(4, 9) G2STEP(5, 9) G2STEP(6, 9) G2STEP(7, 9) G2STEP(8, 9)
    G2STEP(9, 9) G2STEP(10, 9) G2STEP(11, 9) G2STEP(12, 9) G2STEP(13, 9) G2STEP(14, 9) G2STEP(15, 9) G2STEP(16, 9) G2STEP(17, 9)
    G2STEP(18, 9) G2STEP(19, 8) G2STEP(20, 7) G2STEP(21, 6) G2STEP(22, 5) G2STEP(23, 4) G2STEP(24, 3) G2STEP(25, 2) G2STEP(26, 1)
    G2STEP(27, 0)
    STAMP(8);
    f32x16 keep = kk ? acc1 : acc0, give = kk ? acc0 : acc1;
#pragma unroll
    for (int q = 0; q < 4; ++q)
      *(LAS f32x4*)(L_RED + w * 4096 + q * 1024 + lv16) = f32x4{give[4 * q], give[4 * q + 1], give[4 * q + 2], give[4 * q + 3]};
    wait_lgkm0();
    *(volatile LAS u32*)(L_FLG + w * 4) = 1;
    for (u32 spin = 0; spin < (SPIN_CAP << 4); ++spin)
      if (*(volatile LAS u32*)(L_FLG + (w ^ 2) * 4) != 0) break;
    asm volatile("" ::: "memory");
#pragma unroll
    for (int q = 0; q < 4; ++q) {
      f32x4 o = lds_f4(L_RED + (w ^ 2) * 4096 + q * 1024 + lv16);
#pragma unroll
      for (int e = 0; e < 4; ++e) keep[4 * q + e] += o[e];
    }
    STAMP(9);
    const u32 l0 = lane == 0 ? 0u : 0xfffffff0u - WS_BYTES;
    const u32 pslot = m * 4 + g * 2 + kk;
    if (hh == 0) {
      const u32 pdst = WS_P + (pk * 32 + pslot) * 4096;
#pragma unroll
      for (int q = 0; q < 4; ++q)
        st16_sc1(__builtin_bit_cast(v4u, f32x4{keep[4 * q], keep[4 * q + 1], keep[4 * q + 2], keep[4 * q + 3]}), rws, lv16, pdst + q * 1024);
      wait_vm<0>();
      st4_sc1(tag, rws, l0, flg + 1024 + pslot * 16);
      STAMP(10);
    } else {
      const u32 pflag = ctl + 512 + (1 - kh) * 1536 + 1024 + pslot * 16;
      u32 fv;
      for (u32 spin = 0; spin < SPIN_CAP; ++spin) {
        ld4_sc1(fv, rws, 0, pflag);
        wait_vm<0>(fv);
        if (__builtin_amdgcn_readfirstlane(fv) == tag) break;
      }
      STAMP(10);
      const u32 psrc = WS_P + ((pk ^ 1) * 32 + pslot) * 4096;
      v4u pr[4];
#pragma unroll
      for (int q = 0; q < 4; ++q) ld16_sc1(pr[q], rws, lv16, psrc + q * 1024);
      v4u bb0 = *(const LAS v4u*)(L_B2 + kk * 64 + h * 32), bb1 = *(const LAS v4u*)(L_B2 + kk * 64 + h * 32 + 16);
      asm volatile("s_waitcnt vmcnt(0)" : "+v"(pr[0]), "+v"(pr[1]), "+v"(pr[2]), "+v"(pr[3])::"memory");
      v4u o0, o1;
#pragma unroll
      for (int e = 0; e < 4; ++e) {
        o0[e] = pack2(keep[2 * e] + asf(pr[e >> 1][(2 * e) & 3]) + bf_lo(bb0[e]),
                      keep[2 * e + 1] + asf(pr[e >> 1][(2 * e + 1) & 3]) + bf_hi(bb0[e]));
        o1[e] = pack2(keep[8 + 2 * e] + asf(pr[2 + (e >> 1)][(2 * e) & 3]) + bf_lo(bb1[e]),
                      keep[8 + 2 * e + 1] + asf(pr[2 + (e >> 1)][(2 * e + 1) & 3]) + bf_hi(bb1[e]));
      }
      const v4u ro = mk_rsrc(out + panel * (128 * D * 2), 128 * D * 2);
      const u32 ov = (32 * g + t31) * 1024 + h * 32;
      const u32 os = rowO + m * 128 + kk * 64;
      st16(o0, ro, ov, os);
      st16(o1, ro, ov + 16, os);
      if (kh == 0 && m == 0 && w == 4) st4_sc1(tag, rws, l0, ctl);
    }
    STAMP(11);
  }
  wait_vm<0>();
  STAMP(12);
  stamps[15] = stamps[0] + (__builtin_amdgcn_s_memtime() - c0_) * 100 / (stamps[12] - stamps[0]);
  if (blockIdx.x == 77 && w == 4) { unsigned long long until = stamps[0] + 5000 + 10 * (stamps[0] - stamps[0]); while (__builtin_amdgcn_s_memrealtime() < until) __builtin_amdgcn_s_sleep(1); }
}

extern "C" void kernel_launch(void* const* d_in, const int* in_sizes, int n_in, void* d_out, int out_size, void* d_ws,
                              size_t ws_size, hipStream_t stream) {
  (void)in_sizes; (void)n_in; (void)out_size; (void)ws_size;
  (void)hipFuncSetAttribute((const void*)mixer_mlp, hipFuncAttributeMaxDynamicSharedMemorySize, L_BYTES);
  mixer_mlp<<<256, 512, L_BYTES, stream>>>((const char*)d_in[0], (const char*)d_in[1], (const char*)d_in[2], (const char*)d_in[3],
                                           (const char*)d_in[4], (char*)d_out, (char*)d_ws);
}
```

```cpp
#include <hip/hip_runtime.h>
#include <stdint.h>

typedef unsigned u32;
typedef unsigned v2u __attribute__((ext_vector_type(2)));
typedef unsigned v4u __attribute__((ext_vector_type(4)));
typedef short s16x4 __attribute__((ext_vector_type(4)));
typedef __bf16 bf16x2 __attribute__((ext_vector_type(2)));
typedef __bf16 bf16x8 __attribute__((ext_vector_type(8)));
typedef float f32x2 __attribute__((ext_vector_type(2)));
typedef float f32x4 __attribute__((ext_vector_type(4)));
typedef float f32x16 __attribute__((ext_vector_type(16)));
#define LAS __attribute__((address_space(3)))
#define DI __device__ __forceinline__

namespace {
constexpr int D = 512, F = 1792;
constexpr u32 WS_CTL = 0;
constexpr u32 WS_STAT = 1u << 18;
constexpr u32 WS_H = 1u << 20;
constexpr u32 WS_P = 9u << 20;
constexpr u32 WS_BYTES = 13u << 20;
constexpr u32 L_SLOT = 32768;
constexpr u32 L_RED = 114688;
constexpr u32 L_ST = 155648;
constexpr u32 L_B1 = L_ST + 2048;
constexpr u32 L_B2 = L_B1 + 256;
constexpr u32 L_FLG = L_B2 + 256;
constexpr u32 L_JUNK = L_FLG + 256;
constexpr u32 L_BYTES = 163840;
constexpr u32 CODE_LINES = 114;
constexpr u32 SPIN_CAP = 1u << 14;
constexpr u32 OOR = 0x7ffffff0u;

DI v4u mk_rsrc(const void* p, u32 bytes) { return v4u{(u32)(uint64_t)p, (u32)((uint64_t)p >> 32) & 0xffffu, bytes, 0x00020000u}; }
DI void dma16x4(v4u rs, const u32 (&v)[4], u32 soff, u32 lds) {
  asm volatile("s_mov_b32 m0, %6\n\ts_nop 0\n\tbuffer_load_dwordx4 %0, %4, %5 offen lds\n\tbuffer_load_dwordx4 %1, %4, %5 offen offset:1024 lds\n\t"
               "buffer_load_dwordx4 %2, %4, %5 offen offset:2048 lds\n\tbuffer_load_dwordx4 %3, %4, %5 offen offset:3072 lds"
               ::"v"(v[0]), "v"(v[1]), "v"(v[2]), "v"(v[3]), "s"(rs), "s"(soff), "s"(lds) : "memory");
}
DI void dma16x2(v4u rs, const u32 (&v)[2], u32 soff, u32 lds) {
  asm volatile("s_mov_b32 m0, %4\n\ts_nop 0\n\tbuffer_load_dwordx4 %0, %2, %3 offen lds\n\tbuffer_load_dwordx4 %1, %2, %3 offen offset:1024 lds"
               ::"v"(v[0]), "v"(v[1]), "s"(rs), "s"(soff), "s"(lds) : "memory");
}
DI void dma4(v4u rs, u32 voff, u32 soff, u32 lds) {
  asm volatile("s_mov_b32 m0, %3\n\ts_nop 0\n\tbuffer_load_dword %0, %1, %2 offen lds" ::"v"(voff), "s"(rs), "s"(soff), "s"(lds) : "memory");
}
DI void dma4_sc1(v4u rs, u32 voff, u32 soff, u32 lds) {
  asm volatile("s_mov_b32 m0, %3\n\ts_nop 0\n\tbuffer_load_dword %0, %1, %2 offen sc1 lds" ::"v"(voff), "s"(rs), "s"(soff), "s"(lds) : "memory");
}
DI void dma16_sc1(v4u rs, u32 voff, u32 soff, u32 lds) {
  asm volatile("s_mov_b32 m0, %3\n\ts_nop 0\n\tbuffer_load_dwordx4 %0, %1, %2 offen sc1 lds" ::"v"(voff), "s"(rs), "s"(soff), "s"(lds) : "memory");
}
template <int OFF> DI void ld16_sc1(v4u& d, v4u rs, u32 voff, u32 soff) {
  asm volatile("buffer_load_dwordx4 %0, %1, %2, %3 offen offset:%4 sc1" : "=v"(d) : "v"(voff), "s"(rs), "s"(soff), "i"(OFF) : "memory");
}
DI void ld16(v4u& d, v4u rs, u32 voff, u32 soff) {
  asm volatile("buffer_load_dwordx4 %0, %1, %2, %3 offen" : "=v"(d) : "v"(voff), "s"(rs), "s"(soff) : "memory");
}
DI void ld4_sc1(u32& d, v4u rs, u32 voff, u32 soff) {
  asm volatile("buffer_load_dword %0, %1, %2, %3 offen sc1" : "=v"(d) : "v"(voff), "s"(rs), "s"(soff) : "memory");
}
DI void ld16_sc1_w(v4u& d, v4u rs, u32 voff, u32 soff) {
  asm volatile("buffer_load_dwordx4 %0, %1, %2, %3 offen sc1\n\ts_waitcnt vmcnt(0)" : "=&v"(d) : "v"(voff), "s"(rs), "s"(soff) : "memory");
}
DI void ld4_sc1_w(u32& d, v4u rs, u32 voff, u32 soff) {
  asm volatile("buffer_load_dword %0, %1, %2, %3 offen sc1\n\ts_waitcnt vmcnt(0)" : "=&v"(d) : "v"(voff), "s"(rs), "s"(soff) : "memory");
}
DI void ld64_sc1_w(v4u (&d)[4], v4u rs, u32 voff, u32 soff) {
  asm volatile("buffer_load_dwordx4 %0, %4, %5, %6 offen sc1\n\tbuffer_load_dwordx4 %1, %4, %5, %6 offen offset:1024 sc1\n\t"
               "buffer_load_dwordx4 %2, %4, %5, %6 offen offset:2048 sc1\n\tbuffer_load_dwordx4 %3, %4, %5, %6 offen offset:3072 sc1\n\ts_waitcnt vmcnt(0)"
               : "=&v"(d[0]), "=&v"(d[1]), "=&v"(d[2]), "=&v"(d[3]) : "v"(voff), "s"(rs), "s"(soff) : "memory");
}
template <int OFF> DI void st16_sc1(v4u d, v4u rs, u32 voff, u32 soff) {
  asm volatile("buffer_store_dwordx4 %0, %1, %2, %3 offen offset:%4 sc1\n\ts_nop 1" ::"v"(d), "v"(voff), "s"(rs), "s"(soff), "i"(OFF) : "memory");
}
#define OUT_FLAGS " sc0 sc1"
#define POLL_SLEEP 6
DI void st16_out(v4u d, v4u rs, u32 voff, u32 soff) {
  asm volatile("buffer_store_dwordx4 %0, %1, %2, %3 offen" OUT_FLAGS "\n\ts_nop 1" ::"v"(d), "v"(voff), "s"(rs), "s"(soff) : "memory");
}
DI void st4_sc1(u32 d, v4u rs, u32 voff, u32 soff) {
  asm volatile("buffer_store_dword %0, %1, %2, %3 offen sc1" ::"v"(d), "v"(voff), "s"(rs), "s"(soff) : "memory");
}
template <int N> DI void wait_vm() { asm volatile("s_waitcnt vmcnt(%0)" ::"i"(N) : "memory"); }
template <int N, class T> DI void wait_vm(T& r) {
  wait_vm<N>();
  __builtin_amdgcn_sched_barrier(0);
  asm volatile("" : "+v"(r));
}
template <int N> DI void wait_vm4(v4u (&r)[4]) {
  wait_vm<N>();
  __builtin_amdgcn_sched_barrier(0);
  asm volatile("" : "+v"(r[0]), "+v"(r[1]), "+v"(r[2]), "+v"(r[3]));
}
DI void lds_inc_lane0(u32& old, u32 addr, u32 one) {
  asm volatile("s_mov_b64 exec, 1\n\tds_add_rtn_u32 %0, %1, %2\n\ts_mov_b64 exec, -1" : "+v"(old) : "v"(addr), "v"(one) : "memory");
}
DI void wait_lgkm0() { __builtin_amdgcn_s_waitcnt(0xC07F); asm volatile("" ::: "memory"); }
DI void bar() { asm volatile("s_barrier" ::: "memory"); }
DI void sched_fence() { __builtin_amdgcn_sched_barrier(0); }
template <class T> DI void pin(T& v) { asm volatile("" : "+v"(v)); }

DI void pin_s(int& v) { asm volatile("" : "+s"(v)); }
template <int SLEEP> DI void lds_wait_eq(u32 addr, u32 val) {
  u32 t, s, n;
  asm volatile("s_mov_b32 %2, %6\n1:\n\tds_read_b32 %0, %3\n\ts_waitcnt lgkmcnt(0)\n\tv_readfirstlane_b32 %1, %0\n\ts_nop 0\n\ts_cmp_eq_u32 %1, %4\n\t"
               "s_cbranch_scc1 2f\n\ts_sleep %5\n\ts_sub_u32 %2, %2, 1\n\ts_cbranch_scc0 1b\n2:"
               : "=&v"(t), "=&s"(s), "=&s"(n) : "v"(addr), "s"(val), "i"(SLEEP), "i"(SPIN_CAP << 4) : "memory", "scc");
}
template <u32 MASK, int SLEEP> DI void poll_eq(u32& f, v4u rs, u32 voff, u32 soff, u32 tag) {
  u32 n;
  asm volatile("s_mov_b32 %1, %8\n1:\n\tv_cmp_ne_u32 vcc, %5, %0\n\ts_nop 0\n\ts_and_b32 vcc_lo, vcc_lo, %6\n\ts_cbranch_scc0 2f\n\ts_sleep %7\n\t"
               "buffer_load_dword %0, %2, %3, %4 offen sc1\n\ts_sub_u32 %1, %1, 1\n\ts_waitcnt vmcnt(0)\n\ts_cbranch_scc0 1b\n2:"
               : "+v"(f), "=&s"(n) : "v"(voff), "s"(rs), "s"(soff), "s"(tag), "i"(MASK), "i"(SLEEP), "i"(SPIN_CAP) : "memory", "scc", "vcc");
}
DI bf16x8 lds_b128(u32 a) { return *(const LAS bf16x8*)a; }
DI f32x4 lds_f4(u32 a) { return *(const LAS f32x4*)a; }
DI bf16x8 lds_tr(u32 a0, u32 a1) {
  s16x4 lo = __builtin_amdgcn_ds_read_tr16_b64_v4i16((LAS s16x4*)a0);
  s16x4 hi = __builtin_amdgcn_ds_read_tr16_b64_v4i16((LAS s16x4*)a1);
  return __builtin_bit_cast(bf16x8, __builtin_shufflevector(lo, hi, 0, 1, 2, 3, 4, 5, 6, 7));
}
template <bool FIRST> DI void mfma32(f32x16& c, bf16x8 a, bf16x8 b) {
  if (FIRST) asm volatile("v_mfma_f32_32x32x16_bf16 %0, %1, %2, 0" : "=&v"(c) : "v"(a), "v"(b));
  else asm volatile("v_mfma_f32_32x32x16_bf16 %0, %1, %2, %0" : "+v"(c) : "v"(a), "v"(b));
}
template <bool FIRST> DI void mfma16(f32x4& c, bf16x8 a, bf16x8 b) {
  if (FIRST) asm volatile("v_mfma_f32_16x16x32_bf16 %0, %1, %2, 0" : "=&v"(c) : "v"(a), "v"(b));
  else asm volatile("v_mfma_f32_16x16x32_bf16 %0, %1, %2, %0" : "+v"(c) : "v"(a), "v"(b));
}
DI void nop12(f32x16& a, f32x16& b) { asm volatile("s_nop 11" : "+v"(a), "+v"(b)); }
DI u32 pack2(float a, float b) { bf16x2 v = {(__bf16)a, (__bf16)b}; return __builtin_bit_cast(u32, v); }
DI float asf(u32 w) { return __builtin_bit_cast(float, w); }
DI u32 asu(float f) { return __builtin_bit_cast(u32, f); }
DI bf16x2 asb2(u32 w) { return __builtin_bit_cast(bf16x2, w); }
DI float bf_lo(u32 w) { return asf(w << 16); }
DI float bf_hi(u32 w) { return asf(w & 0xffff0000u); }

template <int S> DI v4u gelu8(const f32x16& acc, f32x2 kk, f32x2 sd) {
  f32x2 p[4];
#define PAIR(i) __builtin_shufflevector(acc, acc, 8 * S + 2 * i, 8 * S + 2 * i + 1)
  asm volatile(
      "v_pk_mul_f32 %0, %5, %5\n\tv_pk_mul_f32 %1, %6, %6\n\tv_pk_mul_f32 %2, %7, %7\n\tv_pk_mul_f32 %3, %8, %8\n\t"
      "v_pk_fma_f32 %0, %0, %4, %4 op_sel:[0,0,1] op_sel_hi:[1,0,1]\n\tv_pk_fma_f32 %1, %1, %4, %4 op_sel:[0,0,1] op_sel_hi:[1,0,1]\n\t"
      "v_pk_fma_f32 %2, %2, %4, %4 op_sel:[0,0,1] op_sel_hi:[1,0,1]\n\tv_pk_fma_f32 %3, %3, %4, %4 op_sel:[0,0,1] op_sel_hi:[1,0,1]\n\t"
      "v_pk_mul_f32 %0, %0, %5\n\tv_pk_mul_f32 %1, %1, %6\n\tv_pk_mul_f32 %2, %2, %7\n\tv_pk_mul_f32 %3, %3, %8"
      : "=&v"(p[0]), "=&v"(p[1]), "=&v"(p[2]), "=&v"(p[3])
      : "v"(kk), "v"(PAIR(0)), "v"(PAIR(1)), "v"(PAIR(2)), "v"(PAIR(3)));
#pragma unroll
  for (int i = 0; i < 4; ++i) p[i] = f32x2{__builtin_amdgcn_exp2f(p[i][0]), __builtin_amdgcn_exp2f(p[i][1])};
  sched_fence();
#pragma unroll
  for (int i = 0; i < 4; ++i) p[i] = __builtin_elementwise_fma(p[i], sd, sd);
  sched_fence();
#pragma unroll
  for (int i = 0; i < 4; ++i) p[i] = f32x2{__builtin_amdgcn_rcpf(p[i][0]), __builtin_amdgcn_rcpf(p[i][1])};
  sched_fence();
  p[0] *= PAIR(0); p[1] *= PAIR(1); p[2] *= PAIR(2); p[3] *= PAIR(3);
#undef PAIR
  sched_fence();
  v4u o;
#pragma unroll
  for (int i = 0; i < 4; ++i) o[i] = pack2(p[i][0], p[i][1]);
  return o;
}
DI void split2(float y, u32& hi2, u32& lo2) {
  float r;
  asm volatile("v_cvt_pk_bf16_f32 %0, %3, %3\n\tv_and_b32 %2, 0xffff0000, %0\n\tv_sub_f32 %2, %3, %2\n\tv_cvt_pk_bf16_f32 %1, %2, %2"
               : "=&v"(hi2), "=&v"(lo2), "=&v"(r) : "v"(y));
}
DI void tile_consts(float sd, f32x2& kk) {
  float rs, k1, k2;
  asm volatile("v_rcp_f32 %0, %3\n\ts_nop 0\n\tv_mul_f32 %1, 0xc0135761, %0\n\tv_mul_f32 %2, %0, %0\n\tv_mul_f32 %2, %2, %0\n\tv_mul_f32 %2, 0xbdd2d3e8, %2"
               : "=&v"(rs), "=&v"(k1), "=&v"(k2) : "v"(sd));
  kk = f32x2{k2, k1};
}

#define STAMP(i)
#define FSTAMP(i)
template <int V> struct IC { static constexpr int v = V; };
#define ROT (2 * rg)
#define R7 (m ? j : 0)
#define SW (p >> 1)
#define HELP_W2 4
#define PRETOUCH 1
#define QS (rg + 3 * (p & 1))
#define DF 12
#define DO 36
#define DHF 50
#define DHO 90
#define PUBF 6
#define PRAT 28
#define WINDOW 8
#define HROT rot
}

__global__ __launch_bounds__(512, 2) void mixer_mlp(const char* x, const char* w1, const char* b1p, const char* w2, const char* b2p,
                                                    char* out, char* ws, char* ws2) {
  STAMP(0);
  const u32 lane = threadIdx.x & 63;
  const u32 w = __builtin_amdgcn_readfirstlane(threadIdx.x >> 6) ^ 4;
  const u32 xcd = blockIdx.x & 7, idx = blockIdx.x >> 3;
  const u32 kh = xcd & 1, rg = xcd >> 1, p = idx & 3, panel = rg * 4 + p, m = idx >> 2, j = m - 1;
  const u32 rowF = panel * 131072 + (1 - kh) * 65536, rowO = panel * 131072 + kh * 65536;
  const u32 rot = ROT;
  const u32 a = w & 3;
  if (w == 0) {
    uint64_t pc;
    asm volatile("s_getpc_b64 %0" : "=s"(pc));
    dma4(mk_rsrc((const void*)(pc & ~127ull), CODE_LINES * 128), lane < 5 ? (lane * 32 + idx) * 128 : OOR, 0, L_JUNK);
  }
  u32 xv[4], wv[4];
#pragma unroll
  for (int i = 0; i < 4; ++i) {
    xv[i] = (lane >> 3) * 1024 + (((lane & 7) ^ (lane >> 4) ^ (4 * (i & 1))) * 16) + i * (8192 - 1024);
    wv[i] = (16 * a + (lane >> 4)) * (F * 2) + (((lane & 15) ^ ((lane >> 4) << 2)) * 16) + i * (4 * F * 2 - 1024);
  }
  u32 xs = (a < 2 ? rowF : rowO) + (a & 1) * 32768, w1s = kh * 1792 + j * 256, wl = a * 4096;
  asm volatile("" : "+v"(xv[0]), "+v"(xv[1]), "+v"(xv[2]), "+v"(xv[3]), "+v"(wv[0]), "+v"(wv[1]), "+v"(wv[2]), "+v"(wv[3]));
  asm volatile("" : "+s"(xs), "+s"(w1s), "+s"(wl));
  sched_fence();
  asm volatile("" : "+s"(x), "+s"(w1), "+s"(b1p), "+s"(w2), "+s"(b2p), "+s"(out), "+s"(ws), "+s"(ws2));
  const v4u rx = mk_rsrc(x, 2048 * D * 2);
  const v4u rw1 = mk_rsrc(w1, D * F * 2);
  auto dma_g = [&](u32 kg) {
    const u32 kr = (kg + rot) & 7, sl = (kg & 3) * L_SLOT + ((kg & 3) == 3 ? 16384 : 0) + wl;
    dma16x4(rx, xv, xs + kr * 128, sl);
    dma16x4(rw1, wv, w1s + kr * (64 * F * 2), sl + 16384);
  };
  FSTAMP(1);
  const v4u rws = mk_rsrc(ws, WS_BYTES);
  const u32 epo = WS_CTL + panel * 16384 + 7 * 256;
  if (w >= 4 && m != 0) { dma_g(0); dma_g(1); }
  if (w == 1) dma4_sc1(rws, lane == 0 ? 0u : OOR, epo, L_FLG);
  if (w == 2) dma4(mk_rsrc(b1p, F * 2), lane * 4, m ? w1s : 0u, L_B1);
  if (w == 3) dma4(mk_rsrc(b2p, D * 2), lane * 4, m * 128, L_B2);
  sched_fence();
  const u32 pk = panel * 2 + kh;
  const u32 g = w & 1, c = (w >> 1) & 1;
  const u32 hh = (w >> 2) ^ 1;
  const u32 t31 = lane & 31, h = lane >> 5, q4 = (lane >> 2) & 3;
  const u32 flg = WS_CTL + pk * 8192;
  const u32 sts = WS_STAT + panel * 2048;
  const u32 hpan = WS_H + pk * (4 * 56 * 1024);
  const u32 lv16 = lane * 16;
  const u32 r7 = R7, sw = SW;
  u32 jr[7];
#pragma unroll
  for (int i = 0; i < 7; ++i) jr[i] = i + r7 >= 7 ? i + r7 - 7 : i + r7;
  u32 tag, one = 1;
  pin(one);

  if (m == 0) {
    v4u xr[2][4];
    u32 ep;
    ld4_sc1(ep, rws, 0, epo);
#pragma unroll
    for (int k = 0; k < 8; ++k) ld16(xr[k >> 2][k & 3], rx, xv[0], rowO + w * 8192 + ((k + HROT) & 7) * 128);
    float sum = 0.f, sq = 0.f;
    const bf16x2 ones = {(__bf16)1.0f, (__bf16)1.0f};
    wait_vm<0>(ep);
    wait_vm4<0>(xr[0]);
    wait_vm4<0>(xr[1]);
    tag = __builtin_amdgcn_readfirstlane(ep) + 0x01010101u;
#pragma unroll
    for (int k = 0; k < 8; ++k)
#pragma unroll
      for (int e = 0; e < 4; ++e) {
        const bf16x2 v = asb2(xr[k >> 2][k & 3][e]);
        sum = __builtin_amdgcn_fdot2_f32_bf16(v, ones, sum, false);
        sq = __builtin_amdgcn_fdot2_f32_bf16(v, v, sq, false);
      }
#pragma unroll
    for (int o = 1; o < 8; o <<= 1) { sum += __shfl_xor(sum, o); sq += __shfl_xor(sq, o); }
    const float mean = sum * (1.0f / D);
    const float sd = __builtin_amdgcn_sqrtf(fmaxf(sq * (1.0f / D) - mean * mean, 0.f) + 1e-5f);
    st16_sc1<0>(v4u{asu(-mean), tag, asu(sd), tag}, rws, (lane & 7) ? OOR : (64 * kh + 8 * w + (lane >> 3)) * 16, sts);
    STAMP(1);
    bar();
  }
  if (w >= 4) {
    u32 w2v[2];
    const v4u rw2 = mk_rsrc(w2, F * D * 2);
#pragma unroll
    for (int i = 0; i < 2; ++i) w2v[i] = (16 * a + (lane >> 3)) * 1024 + (((lane & 7) ^ ((lane >> 2) & 4)) * 16) + i * (8192 - 1024);
    const u32 w2s = kh * 896 * 1024 + m * 128;
    int q = QS, jj = q + (int)r7;
    jj = jj >= 7 ? jj - 7 : jj;
    auto dma_w2 = [&](int) {
      pin_s(q); pin_s(jj);
      dma16x2(rw2, w2v, w2s + (q >= 7 ? (sw ^ 1) * 65536 : sw * 65536) + jj * 131072, q * 8192 + a * 2048);
      q = q == 13 ? 0 : q + 1;
      jj = jj == 6 ? 0 : jj + 1;
    };
    if (m != 0) {
#pragma nounroll
      for (u32 kg = 0; kg < 6; ++kg) {
        wait_vm<8>();
        bar();
        if (kg == 5) dma16_sc1(rws, lv16, sts + (a & 1) * 1024, L_ST + (a & 1) * 1024);
        dma_g(kg + 2);
      }
      wait_vm<8>();
      bar();
      tag = __builtin_amdgcn_readfirstlane(*(const LAS u32*)L_FLG) + 0x01010101u;
    }
    else {
      const u32 pv[4] = {lv16, lv16, lv16, lv16};
#pragma nounroll
      for (u32 i = 0; i < HELP_W2; ++i) {
        u32 rb = rg * 14 + i * 4 + a;
        rb = rb >= 56 ? rb - 56 : rb;
        dma16x4(rw2, pv, kh * 896 * 1024 + (p * 56 + rb) * 4096, L_RED + a * 4096);
      }
    }
    dma_w2(0); dma_w2(1);
    if (m != 0) {
      wait_vm<4>();
      bar();
      if (m == 1 && w == 4) st4_sc1(tag, rws, lane == 0 ? 0u : OOR, flg + 16 * 256);
    }
    dma_w2(2); dma_w2(3); dma_w2(4); dma_w2(5); dma_w2(6); dma_w2(7);
    dma_w2(8); dma_w2(9); dma_w2(10); dma_w2(11); dma_w2(12); dma_w2(13);
    u32 old = 0;
    wait_vm<0>();
    lds_inc_lane0(old, L_FLG + 80, one);
    asm volatile("s_waitcnt lgkmcnt(0)" : "+v"(old)::"memory");
    STAMP(6);
    __builtin_amdgcn_s_setprio(2);
  } else {
    if (m != 0) {
      if (w == 0) {
        const u32 tv = lane == 0 ? hpan : lane == 1 ? hpan + 4 * 56 * 1024 - 128 : lane == 2 ? WS_P + (pk * 32 + m * 4) * 4096 :
                       lane == 3 ? WS_P + ((pk ^ 1) * 32 + m * 4) * 4096 : (PRETOUCH > 1 && lane < 6) ? flg + (lane - 4) * 2048 + j * 256 : OOR;
        dma4(rws, tv, 0, L_JUNK);
        dma4(mk_rsrc(out, 2048 * D * 2), lane == 0 ? rowO : OOR, 0, L_JUNK);
      }
      u32 xa[2][4], wa[2];
#pragma unroll
      for (int k = 0; k < 4; ++k) {
        xa[0][k] = t31 * 128 + (((2 * k + h) ^ ((t31 >> 1) & 7)) * 16);
        xa[1][k] = xa[0][k] + 2 * L_SLOT;
        pin(xa[0][k]); pin(xa[1][k]);
      }
      wa[0] = 16384 + (8 * h + q4) * 256 + ((w ^ q4) * 64) + ((lane >> 4) & 1) * 32 + (lane & 3) * 8;
      wa[1] = wa[0] + 2 * L_SLOT;
      pin(wa[0]); pin(wa[1]);
      const u32 selw = ((lane & 3) == 0 && ((lane >> 2) & 1) == ((lane >> 4) & 1)) ? 0x3f803f80u : 0u;
      v4u selv = {selw, selw, selw, selw};
      pin(selv);
      const bf16x8 sel = __builtin_bit_cast(bf16x8, selv);
      u32 sta = L_ST + t31 * 16, b1a = L_B1 + w * 64 + t31 * 2;
      pin(sta); pin(b1a);
      bf16x8 xf[4][4], wf[4];
      f32x16 acc[4];
      f32x4 cs;
      v4u st[4], bfr[4];
      f32x2 kk[4], sd[4];
      u32 ep, b1w;
      auto rd = [&](auto N, auto PART) {
        constexpr int kg = N.v >> 2, k = N.v & 3;
        constexpr u32 sl = (kg & 1) * L_SLOT + ((kg & 3) == 3 ? 16384 : 0);
        if (PART.v == 0) wf[k] = lds_tr(wa[(kg >> 1) & 1] + sl + k * 4096, wa[(kg >> 1) & 1] + sl + k * 4096 + 1024);
        else {
          xf[k][2 * PART.v - 2] = lds_b128(xa[(kg >> 1) & 1][k] + sl + (2 * PART.v - 2) * 4096);
          xf[k][2 * PART.v - 1] = lds_b128(xa[(kg >> 1) & 1][k] + sl + (2 * PART.v - 1) * 4096);
        }
      };
      auto prep = [&](auto T) {
        const u32 nm = st[T.v].x, sdv = st[T.v].z;
        u32 hi2, lo2;
        split2(asf(h ? sdv : nm), hi2, lo2);
        const u32 y = (hi2 & 0xffffu) | (lo2 & 0xffff0000u);
        bfr[T.v] = v4u{y, y, 0u, 0u};
        tile_consts(asf(sdv), kk[T.v]);
        sd[T.v] = f32x2{asf(sdv), asf(sdv)};
        pin(bfr[T.v]); pin(sd[T.v]); pin(kk[T.v]);
      };
      auto step = [&](auto N) {
        constexpr int n = N.v, mmn = n - 2, k = mmn & 3;
        if constexpr (mmn >= 0) mfma32<mmn == 0>(acc[0], wf[k], xf[k][0]);
        sched_fence();
        if constexpr (n < 32) rd(N, IC<0>{});
        sched_fence();
        if constexpr (mmn >= 0) mfma32<mmn == 0>(acc[1], wf[k], xf[k][1]);
        sched_fence();
        if constexpr (n < 32) rd(N, IC<1>{});
        sched_fence();
        if constexpr (mmn >= 0) mfma32<mmn == 0>(acc[2], wf[k], xf[k][2]);
        sched_fence();
        if constexpr (n < 32) rd(N, IC<2>{});
        sched_fence();
        if constexpr (mmn >= 0) {
          mfma32<mmn == 0>(acc[3], wf[k], xf[k][3]);
          sched_fence();
          if constexpr (n >= 28 && n < 32) prep(IC<n - 28>{});
          sched_fence();
          if constexpr (mmn < 30) mfma16<mmn == 0>(cs, sel, wf[k]);
        }
        sched_fence();
      };
      auto iter = [&](auto KG) {
        constexpr int kg = KG.v;
        if (kg == 2) wait_vm<0>();
        wait_lgkm0();
        bar();
        if (kg == 0) STAMP(1);
        FSTAMP(2 + kg);
        step(IC<4 * kg>{});
        if constexpr (kg == 3) { ep = *(const LAS u32*)L_FLG; b1w = *(const LAS unsigned short*)b1a; sched_fence(); }
        if constexpr (kg == 6) {
#pragma unroll
          for (int t = 0; t < 4; ++t) st[t] = *(const LAS v4u*)(sta + (((t >> 1) ^ kh ^ 1) * 2 + (t & 1)) * 512);
          sched_fence();
        }
        step(IC<4 * kg + 1>{}); step(IC<4 * kg + 2>{}); step(IC<4 * kg + 3>{});
        if constexpr (kg == 6) {
          tag = __builtin_amdgcn_readfirstlane(ep) + 0x01010101u;
#pragma nounroll
          for (u32 spin = 0; spin < SPIN_CAP; ++spin) {
            u32 bad = 0;
#pragma unroll
            for (int t = 0; t < 4; ++t) bad |= (st[t].y ^ tag) | (st[t].w ^ tag);
            if (__builtin_amdgcn_ballot_w64(bad != 0) == 0) break;
#pragma unroll
            for (int t = 0; t < 4; ++t) ld16_sc1_w(st[t], rws, t31 * 16, sts + (((t >> 1) ^ kh ^ 1) * 2 + (t & 1)) * 512);
          }
        }
      };
      iter(IC<0>{}); iter(IC<1>{}); iter(IC<2>{}); iter(IC<3>{}); iter(IC<4>{}); iter(IC<5>{}); iter(IC<6>{}); iter(IC<7>{});
      FSTAMP(10);
      mfma16<false>(cs, sel, wf[2]);
      mfma16<false>(cs, sel, wf[3]);
      step(IC<32>{});
      asm volatile("s_nop 3" : "+v"(cs));
      v4u afr;
      {
        u32 hi2, lo2;
        split2(h ? bf_lo(b1w) : cs[0], hi2, lo2);
        afr = v4u{hi2, lo2, 0u, 0u};
        pin(afr);
      }
      sched_fence();
      step(IC<33>{});
#pragma unroll
      for (int t = 0; t < 4; ++t) mfma32<false>(acc[t], __builtin_bit_cast(bf16x8, afr), __builtin_bit_cast(bf16x8, bfr[t]));
      __builtin_amdgcn_s_setprio(3);
      const u32 hdst = hpan + (w >> 1) * (28 * 1024) + j * 4096 + (w & 1) * 2048;
      u32 tagv = tag, cnt = L_FLG + 64;
      pin(tagv); pin(cnt);
      asm volatile("s_nop 8" : "+v"(acc[0]), "+v"(acc[1]), "+v"(acc[2]), "+v"(acc[3]));
      STAMP(2);
      FSTAMP(11);
      auto signal = [&](u32& old, u32 stepi) {
        asm volatile("s_waitcnt lgkmcnt(0)" ::: "memory");
        sched_fence();
        pin(old);
        st4_sc1(tagv, rws, __builtin_amdgcn_readfirstlane(old) == 3 && lane < 32 ? lane * 4 : OOR, flg + stepi * 2048 + j * 256);
      };
      auto gfrag = [&](auto N) {
        constexpr int t = N.v >> 1, s = N.v & 1;
        v4u o = gelu8<s>(acc[t], kk[t], sd[t]);
        u32 old = 0;
        pin(o);
        if (N.v == PUBF) { wait_vm<PUBF - 4>(); lds_inc_lane0(old, cnt, one); signal(old, 0); }
        st16_sc1<s * 1024>(o, rws, lv16, hdst + t * (56 * 1024));
      };
      gfrag(IC<0>{}); gfrag(IC<1>{}); gfrag(IC<2>{}); gfrag(IC<3>{});
      STAMP(3);
      gfrag(IC<4>{}); gfrag(IC<5>{}); gfrag(IC<6>{});
      STAMP(4);
      gfrag(IC<7>{});
      STAMP(5);
      u32 old = 0;
      wait_vm<0>();
      lds_inc_lane0(old, cnt + 4, one);
      signal(old, 1);
      __builtin_amdgcn_s_setprio(0);
      STAMP(6);
    }
  }

  {
    const u32 pollv = lane < 7 ? lane * 256 : OOR, polls = flg + hh * 2048;
    const u32 hsrc = hpan + (hh * 2 + g) * (56 * 1024) + c * (28 * 1024);
    const u32 p4 = lane & 3, blk = (lane >> 4) & 1;
    u32 a0 = (c ^ sw) * 57344 + (4 * h + q4) * 128 + (((q4 >> 1) ^ c) * 64) + (4 * (p4 & 1) + 2 * blk + (p4 >> 1)) * 8;
    u32 a1 = a0 ^ 64;
    pin(a0); pin(a1);
    u32 fv;
    if (m == 0) {
      fv = ~tag;
      poll_eq<1, 4>(fv, rws, 0, flg + 16 * 256, tag);
      if (hh) __builtin_amdgcn_s_sleep(DHO - DHF);
      __builtin_amdgcn_s_sleep(DHF - POLL_SLEEP);
      fv = ~tag;
      poll_eq<127, POLL_SLEEP>(fv, rws, pollv, polls, tag);
    } else {
      lds_wait_eq<1>(L_FLG + 64 + hh * 4, 4);
    }
    STAMP(7);
    v4u hf[28], pr[4];
    auto ld_h = [&](auto I) { ld16_sc1<(I.v & 3) * 1024>(hf[I.v], rws, lv16, hsrc + jr[I.v >> 2] * 4096); };
    constexpr int WIN = WINDOW;
    ld_h(IC<0>{}); ld_h(IC<1>{}); ld_h(IC<2>{}); ld_h(IC<3>{});
    f32x16 acc0, acc1;
    bf16x8 wa0[3], wa1[3];
    auto rd_w = [&](auto I, auto N) {
      if (N.v) wa1[I.v % 3] = lds_tr(a1 + I.v * 2048, a1 + I.v * 2048 + 1024);
      else wa0[I.v % 3] = lds_tr(a0 + I.v * 2048, a0 + I.v * 2048 + 1024);
    };
    lds_wait_eq<0>(L_FLG + 80, 4);
    rd_w(IC<0>{}, IC<1>{}); rd_w(IC<0>{}, IC<0>{}); rd_w(IC<1>{}, IC<1>{}); rd_w(IC<1>{}, IC<0>{});
    if (m != 0) { if (hh) __builtin_amdgcn_s_sleep(DO); else __builtin_amdgcn_s_sleep(DF); }
    ld4_sc1(fv, rws, pollv, polls);
    const u32 pslot = m * 4 + g * 2 + c;
    const u32 psrc = WS_P + ((pk ^ 1) * 32 + pslot) * 4096;
    auto g2 = [&](auto I) {
      constexpr int i = I.v;
      constexpr int younger = i < 4 ? 4 - i : i + WIN < 28 ? WIN - 1 : 27 - i;
      wait_vm<younger>(hf[i]);
      mfma32<i == 0>(acc1, wa1[i % 3], __builtin_bit_cast(bf16x8, hf[i]));
      sched_fence();
      if constexpr (i >= 4 && i + WIN < 28) ld_h(IC<i + WIN>{});
      if constexpr (i + 2 < 28) rd_w(IC<i + 2>{}, IC<1>{});
      sched_fence();
      mfma32<i == 0>(acc0, wa0[i % 3], __builtin_bit_cast(bf16x8, hf[i]));
      sched_fence();
      if constexpr (i + 2 < 28) rd_w(IC<i + 2>{}, IC<0>{});
      sched_fence();
    };
    g2(IC<0>{}); g2(IC<1>{}); g2(IC<2>{}); g2(IC<3>{});
    wait_vm<0>(fv);
    poll_eq<127, POLL_SLEEP>(fv, rws, pollv, polls, tag);
    STAMP(8);
    ld_h(IC<4>{}); ld_h(IC<5>{}); ld_h(IC<6>{}); ld_h(IC<7>{}); ld_h(IC<8>{}); ld_h(IC<9>{});
    if constexpr (WIN > 6) { ld_h(IC<10>{}); ld_h(IC<11>{}); }
    static_assert(WIN == 6 || WIN == 8, "");
    g2(IC<4>{}); g2(IC<5>{}); g2(IC<6>{});
    g2(IC<7>{}); g2(IC<8>{}); g2(IC<9>{}); g2(IC<10>{}); g2(IC<11>{}); g2(IC<12>{}); g2(IC<13>{});
    g2(IC<14>{}); g2(IC<15>{}); g2(IC<16>{}); g2(IC<17>{}); g2(IC<18>{}); g2(IC<19>{}); g2(IC<20>{});
    g2(IC<21>{}); g2(IC<22>{}); g2(IC<23>{}); g2(IC<24>{}); g2(IC<25>{}); g2(IC<26>{}); g2(IC<27>{});
    STAMP(9);
    const u32 red = L_RED + (w ^ 2) * 4096;
    auto exchange = [&]() {
      nop12(acc0, acc1);
#pragma unroll
      for (int q = 0; q < 4; ++q)
        *(LAS f32x4*)(L_RED + w * 4096 + q * 1024 + lv16) = f32x4{acc1[4 * q], acc1[4 * q + 1], acc1[4 * q + 2], acc1[4 * q + 3]};
      wait_lgkm0();
      *(volatile LAS u32*)(L_FLG + 32 + w * 4) = 1;
      lds_wait_eq<0>(L_FLG + 32 + (w ^ 2) * 4, 1);
#pragma unroll
      for (int q = 0; q < 4; ++q) {
        f32x4 o = lds_f4(red + q * 1024 + lv16);
#pragma unroll
        for (int e = 0; e < 4; ++e) acc0[4 * q + e] += o[e];
      }
    };
    if (hh) { ld16_sc1<0>(pr[0], rws, lv16, psrc); ld16_sc1<1024>(pr[1], rws, lv16, psrc); ld16_sc1<2048>(pr[2], rws, lv16, psrc); ld16_sc1<3072>(pr[3], rws, lv16, psrc); }
    exchange();
    STAMP(10);
    if (hh == 0) {
      const u32 pdst = WS_P + (pk * 32 + pslot) * 4096;
      v4u d[4];
#pragma unroll
      for (int q = 0; q < 4; ++q)
#pragma unroll
        for (int e = 0; e < 4; ++e) d[q][e] = (asu(acc0[4 * q + e]) & 0xffffff00u) | ((tag >> (8 * e)) & 0xffu);
      st16_sc1<0>(d[0], rws, lv16, pdst); st16_sc1<1024>(d[1], rws, lv16, pdst); st16_sc1<2048>(d[2], rws, lv16, pdst); st16_sc1<3072>(d[3], rws, lv16, pdst);
      STAMP(11);
    } else {
      const v4u bb0 = *(const LAS v4u*)(L_B2 + c * 64 + h * 32), bb1 = *(const LAS v4u*)(L_B2 + c * 64 + h * 32 + 16);
      wait_vm4<0>(pr);
#pragma nounroll
      for (u32 spin = 0;; ++spin) {
        u32 bad = 0;
#pragma unroll
        for (int q = 0; q < 4; ++q)
#pragma unroll
          for (int e = 0; e < 4; ++e) bad |= pr[q][e] ^ (tag >> (8 * e));
        if (__builtin_amdgcn_ballot_w64((bad & 0xffu) != 0) == 0 || spin >= SPIN_CAP) break;
        __builtin_amdgcn_s_sleep(POLL_SLEEP);
        ld64_sc1_w(pr, rws, lv16, psrc);
      }
      STAMP(11);
      v4u o0, o1;
#pragma unroll
      for (int e = 0; e < 4; ++e) {
        o0[e] = pack2(acc0[2 * e] + asf(pr[e >> 1][(2 * e) & 3] & 0xffffff00u) + bf_lo(bb0[e]),
                      acc0[2 * e + 1] + asf(pr[e >> 1][(2 * e + 1) & 3] & 0xffffff00u) + bf_hi(bb0[e]));
        o1[e] = pack2(acc0[8 + 2 * e] + asf(pr[2 + (e >> 1)][(2 * e) & 3] & 0xffffff00u) + bf_lo(bb1[e]),
                      acc0[8 + 2 * e + 1] + asf(pr[2 + (e >> 1)][(2 * e + 1) & 3] & 0xffffff00u) + bf_hi(bb1[e]));
      }
      const u32 ta = red + t31 * 64 + (((2 * h) ^ ((t31 >> 1) & 3)) * 16);
      *(LAS v4u*)ta = o0;
      *(LAS v4u*)(ta ^ 16) = o1;
      const u32 tb = red + (lane >> 2) * 64 + (((lane & 3) ^ ((lane >> 3) & 3)) * 16);
      o0 = *(const LAS v4u*)tb;
      o1 = *(const LAS v4u*)(tb + 1024);
      const v4u ro = mk_rsrc(out, 2048 * D * 2);
      const u32 ov = (32 * g + (lane >> 2)) * 1024 + (lane & 3) * 16;
      const u32 os = rowO + m * 128 + c * 64;
      st16_out(o0, ro, ov, os);
      st16_out(o1, ro, ov, os + 16 * 1024);
      if (kh == 0 && m == 0 && w == 0) st4_sc1(tag, rws, lane == 0 ? 0u : OOR, epo);
    }
    STAMP(12);
  }
}

extern "C" void kernel_launch(void* const* d_in, const int* in_sizes, int n_in, void* d_out, int out_size, void* d_ws,
                              size_t ws_size, hipStream_t stream) {
  (void)in_sizes; (void)n_in; (void)out_size; (void)ws_size;
  (void)hipFuncSetAttribute((const void*)mixer_mlp, hipFuncAttributeMaxDynamicSharedMemorySize, L_BYTES);
  mixer_mlp<<<256, 512, L_BYTES, stream>>>((const char*)d_in[0], (const char*)d_in[1], (const char*)d_in[2], (const char*)d_in[3],
                                           (const char*)d_in[4], (char*)d_out, (char*)d_ws, (char*)d_ws);
}
```

```cpp
#define STAMPS
#include <hip/hip_runtime.h>
#include <stdint.h>

typedef unsigned u32;
typedef unsigned v2u __attribute__((ext_vector_type(2)));
typedef unsigned v4u __attribute__((ext_vector_type(4)));
typedef short s16x4 __attribute__((ext_vector_type(4)));
typedef __bf16 bf16x2 __attribute__((ext_vector_type(2)));
typedef __bf16 bf16x8 __attribute__((ext_vector_type(8)));
typedef float f32x2 __attribute__((ext_vector_type(2)));
typedef float f32x4 __attribute__((ext_vector_type(4)));
typedef float f32x16 __attribute__((ext_vector_type(16)));
#define LAS __attribute__((address_space(3)))
#define DI __device__ __forceinline__

namespace {
constexpr int D = 512, F = 1792;
constexpr u32 WS_CTL = 0;
constexpr u32 WS_STAT = 1u << 18;
constexpr u32 WS_H = 1u << 20;
constexpr u32 WS_P = 9u << 20;
constexpr u32 WS_BYTES = 13u << 20;
constexpr u32 L_SLOT = 32768;
constexpr u32 L_RED = 114688;
constexpr u32 L_ST = 155648;
constexpr u32 L_B1 = L_ST + 2048;
constexpr u32 L_B2 = L_B1 + 256;
constexpr u32 L_FLG = L_B2 + 256;
constexpr u32 L_JUNK = L_FLG + 256;
constexpr u32 L_BYTES = 163840;
constexpr u32 CODE_LINES = 118;
constexpr u32 SPIN_CAP = 1u << 14;
constexpr u32 OOR = 0x7ffffff0u;

DI v4u mk_rsrc(const void* p, u32 bytes) { return v4u{(u32)(uint64_t)p, (u32)((uint64_t)p >> 32) & 0xffffu, bytes, 0x00020000u}; }
DI void dma16x4(v4u rs, const u32 (&v)[4], u32 soff, u32 lds) {
  asm volatile("s_mov_b32 m0, %6\n\ts_nop 0\n\tbuffer_load_dwordx4 %0, %4, %5 offen lds\n\tbuffer_load_dwordx4 %1, %4, %5 offen offset:1024 lds\n\t"
               "buffer_load_dwordx4 %2, %4, %5 offen offset:2048 lds\n\tbuffer_load_dwordx4 %3, %4, %5 offen offset:3072 lds"
               ::"v"(v[0]), "v"(v[1]), "v"(v[2]), "v"(v[3]), "s"(rs), "s"(soff), "s"(lds) : "memory");
}
DI void dma16x2(v4u rs, const u32 (&v)[2], u32 soff, u32 lds) {
  asm volatile("s_mov_b32 m0, %4\n\ts_nop 0\n\tbuffer_load_dwordx4 %0, %2, %3 offen lds\n\tbuffer_load_dwordx4 %1, %2, %3 offen offset:1024 lds"
               ::"v"(v[0]), "v"(v[1]), "s"(rs), "s"(soff), "s"(lds) : "memory");
}
DI void dma4(v4u rs, u32 voff, u32 soff, u32 lds) {
  asm volatile("s_mov_b32 m0, %3\n\ts_nop 0\n\tbuffer_load_dword %0, %1, %2 offen lds" ::"v"(voff), "s"(rs), "s"(soff), "s"(lds) : "memory");
}
DI void dma4_sc1(v4u rs, u32 voff, u32 soff, u32 lds) {
  asm volatile("s_mov_b32 m0, %3\n\ts_nop 0\n\tbuffer_load_dword %0, %1, %2 offen sc1 lds" ::"v"(voff), "s"(rs), "s"(soff), "s"(lds) : "memory");
}
DI void dma16_sc1(v4u rs, u32 voff, u32 soff, u32 lds) {
  asm volatile("s_mov_b32 m0, %3\n\ts_nop 0\n\tbuffer_load_dwordx4 %0, %1, %2 offen sc1 lds" ::"v"(voff), "s"(rs), "s"(soff), "s"(lds) : "memory");
}
template <int OFF> DI void ld16_sc1(v4u& d, v4u rs, u32 voff, u32 soff) {
  asm volatile("buffer_load_dwordx4 %0, %1, %2, %3 offen offset:%4 sc1" : "=v"(d) : "v"(voff), "s"(rs), "s"(soff), "i"(OFF) : "memory");
}
DI void ld16(v4u& d, v4u rs, u32 voff, u32 soff) {
  asm volatile("buffer_load_dwordx4 %0, %1, %2, %3 offen" : "=v"(d) : "v"(voff), "s"(rs), "s"(soff) : "memory");
}
DI void ld4_sc1(u32& d, v4u rs, u32 voff, u32 soff) {
  asm volatile("buffer_load_dword %0, %1, %2, %3 offen sc1" : "=v"(d) : "v"(voff), "s"(rs), "s"(soff) : "memory");
}
DI void ld16_sc1_w(v4u& d, v4u rs, u32 voff, u32 soff) {
  asm volatile("buffer_load_dwordx4 %0, %1, %2, %3 offen sc1\n\ts_waitcnt vmcnt(0)" : "=&v"(d) : "v"(voff), "s"(rs), "s"(soff) : "memory");
}
DI void ld4_sc1_w(u32& d, v4u rs, u32 voff, u32 soff) {
  asm volatile("buffer_load_dword %0, %1, %2, %3 offen sc1\n\ts_waitcnt vmcnt(0)" : "=&v"(d) : "v"(voff), "s"(rs), "s"(soff) : "memory");
}
DI void ld64_sc1_w(v4u (&d)[4], v4u rs, u32 voff, u32 soff) {
  asm volatile("buffer_load_dwordx4 %0, %4, %5, %6 offen sc1\n\tbuffer_load_dwordx4 %1, %4, %5, %6 offen offset:1024 sc1\n\t"
               "buffer_load_dwordx4 %2, %4, %5, %6 offen offset:2048 sc1\n\tbuffer_load_dwordx4 %3, %4, %5, %6 offen offset:3072 sc1\n\ts_waitcnt vmcnt(0)"
               : "=&v"(d[0]), "=&v"(d[1]), "=&v"(d[2]), "=&v"(d[3]) : "v"(voff), "s"(rs), "s"(soff) : "memory");
}
template <int OFF> DI void st16_sc1(v4u d, v4u rs, u32 voff, u32 soff) {
  asm volatile("buffer_store_dwordx4 %0, %1, %2, %3 offen offset:%4 sc1\n\ts_nop 1" ::"v"(d), "v"(voff), "s"(rs), "s"(soff), "i"(OFF) : "memory");
}
#define OUT_FLAGS " sc0 sc1"
#define POLL_SLEEP 6
DI void st16_out(v4u d, v4u rs, u32 voff, u32 soff) {
  asm volatile("buffer_store_dwordx4 %0, %1, %2, %3 offen" OUT_FLAGS "\n\ts_nop 1" ::"v"(d), "v"(voff), "s"(rs), "s"(soff) : "memory");
}
DI void st4_sc1(u32 d, v4u rs, u32 voff, u32 soff) {
  asm volatile("buffer_store_dword %0, %1, %2, %3 offen sc1" ::"v"(d), "v"(voff), "s"(rs), "s"(soff) : "memory");
}
template <int N> DI void wait_vm() { asm volatile("s_waitcnt vmcnt(%0)" ::"i"(N) : "memory"); }
template <int N, class T> DI void wait_vm(T& r) {
  wait_vm<N>();
  __builtin_amdgcn_sched_barrier(0);
  asm volatile("" : "+v"(r));
}
template <int N> DI void wait_vm4(v4u (&r)[4]) {
  wait_vm<N>();
  __builtin_amdgcn_sched_barrier(0);
  asm volatile("" : "+v"(r[0]), "+v"(r[1]), "+v"(r[2]), "+v"(r[3]));
}
DI void lds_inc_lane0(u32& old, u32 addr, u32 one) {
  asm volatile("s_mov_b64 exec, 1\n\tds_add_rtn_u32 %0, %1, %2\n\ts_mov_b64 exec, -1" : "+v"(old) : "v"(addr), "v"(one) : "memory");
}
DI void wait_lgkm0() { __builtin_amdgcn_s_waitcnt(0xC07F); asm volatile("" ::: "memory"); }
DI void bar() { asm volatile("s_barrier" ::: "memory"); }
DI void sched_fence() { __builtin_amdgcn_sched_barrier(0); }
template <class T> DI void pin(T& v) { asm volatile("" : "+v"(v)); }

DI void pin_s(int& v) { asm volatile("" : "+s"(v)); }
template <int SLEEP> DI void lds_wait_eq(u32 addr, u32 val) {
  u32 t, s, n;
  asm volatile("s_mov_b32 %2, %6\n1:\n\tds_read_b32 %0, %3\n\ts_waitcnt lgkmcnt(0)\n\tv_readfirstlane_b32 %1, %0\n\ts_nop 0\n\ts_cmp_eq_u32 %1, %4\n\t"
               "s_cbranch_scc1 2f\n\ts_sleep %5\n\ts_sub_u32 %2, %2, 1\n\ts_cbranch_scc0 1b\n2:"
               : "=&v"(t), "=&s"(s), "=&s"(n) : "v"(addr), "s"(val), "i"(SLEEP), "i"(SPIN_CAP << 4) : "memory", "scc");
}
template <u32 MASK, int SLEEP> DI void poll_eq(u32& f, v4u rs, u32 voff, u32 soff, u32 tag) {
  u32 n;
  asm volatile("s_mov_b32 %1, %8\n1:\n\tv_cmp_ne_u32 vcc, %5, %0\n\ts_nop 0\n\ts_and_b32 vcc_lo, vcc_lo, %6\n\ts_cbranch_scc0 2f\n\ts_sleep %7\n\t"
               "buffer_load_dword %0, %2, %3, %4 offen sc1\n\ts_sub_u32 %1, %1, 1\n\ts_waitcnt vmcnt(0)\n\ts_cbranch_scc0 1b\n2:"
               : "+v"(f), "=&s"(n) : "v"(voff), "s"(rs), "s"(soff), "s"(tag), "i"(MASK), "i"(SLEEP), "i"(SPIN_CAP) : "memory", "scc", "vcc");
}
DI bf16x8 lds_b128(u32 a) { return *(const LAS bf16x8*)a; }
DI f32x4 lds_f4(u32 a) { return *(const LAS f32x4*)a; }
DI bf16x8 lds_tr(u32 a0, u32 a1) {
  s16x4 lo = __builtin_amdgcn_ds_read_tr16_b64_v4i16((LAS s16x4*)a0);
  s16x4 hi = __builtin_amdgcn_ds_read_tr16_b64_v4i16((LAS s16x4*)a1);
  return __builtin_bit_cast(bf16x8, __builtin_shufflevector(lo, hi, 0, 1, 2, 3, 4, 5, 6, 7));
}
template <bool FIRST> DI void mfma32(f32x16& c, bf16x8 a, bf16x8 b) {
  if (FIRST) asm volatile("v_mfma_f32_32x32x16_bf16 %0, %1, %2, 0" : "=&v"(c) : "v"(a), "v"(b));
  else asm volatile("v_mfma_f32_32x32x16_bf16 %0, %1, %2, %0" : "+v"(c) : "v"(a), "v"(b));
}
template <bool FIRST> DI void mfma16(f32x4& c, bf16x8 a, bf16x8 b) {
  if (FIRST) asm volatile("v_mfma_f32_16x16x32_bf16 %0, %1, %2, 0" : "=&v"(c) : "v"(a), "v"(b));
  else asm volatile("v_mfma_f32_16x16x32_bf16 %0, %1, %2, %0" : "+v"(c) : "v"(a), "v"(b));
}
DI void nop12(f32x16& a, f32x16& b) { asm volatile("s_nop 11" : "+v"(a), "+v"(b)); }
DI u32 pack2(float a, float b) { bf16x2 v = {(__bf16)a, (__bf16)b}; return __builtin_bit_cast(u32, v); }
DI float asf(u32 w) { return __builtin_bit_cast(float, w); }
DI u32 asu(float f) { return __builtin_bit_cast(u32, f); }
DI bf16x2 asb2(u32 w) { return __builtin_bit_cast(bf16x2, w); }
DI float bf_lo(u32 w) { return asf(w << 16); }
DI float bf_hi(u32 w) { return asf(w & 0xffff0000u); }

template <int S> DI v4u gelu8(const f32x16& acc, f32x2 kk, f32x2 sd) {
  f32x2 p[4];
#define PAIR(i) __builtin_shufflevector(acc, acc, 8 * S + 2 * i, 8 * S + 2 * i + 1)
  asm volatile(
      "v_pk_mul_f32 %0, %5, %5\n\tv_pk_mul_f32 %1, %6, %6\n\tv_pk_mul_f32 %2, %7, %7\n\tv_pk_mul_f32 %3, %8, %8\n\t"
      "v_pk_fma_f32 %0, %0, %4, %4 op_sel:[0,0,1] op_sel_hi:[1,0,1]\n\tv_pk_fma_f32 %1, %1, %4, %4 op_sel:[0,0,1] op_sel_hi:[1,0,1]\n\t"
      "v_pk_fma_f32 %2, %2, %4, %4 op_sel:[0,0,1] op_sel_hi:[1,0,1]\n\tv_pk_fma_f32 %3, %3, %4, %4 op_sel:[0,0,1] op_sel_hi:[1,0,1]\n\t"
      "v_pk_mul_f32 %0, %0, %5\n\tv_pk_mul_f32 %1, %1, %6\n\tv_pk_mul_f32 %2, %2, %7\n\tv_pk_mul_f32 %3, %3, %8"
      : "=&v"(p[0]), "=&v"(p[1]), "=&v"(p[2]), "=&v"(p[3])
      : "v"(kk), "v"(PAIR(0)), "v"(PAIR(1)), "v"(PAIR(2)), "v"(PAIR(3)));
#pragma unroll
  for (int i = 0; i < 4; ++i) p[i] = f32x2{__builtin_amdgcn_exp2f(p[i][0]), __builtin_amdgcn_exp2f(p[i][1])};
  sched_fence();
#pragma unroll
  for (int i = 0; i < 4; ++i) p[i] = __builtin_elementwise_fma(p[i], sd, sd);
  sched_fence();
#pragma unroll
  for (int i = 0; i < 4; ++i) p[i] = f32x2{__builtin_amdgcn_rcpf(p[i][0]), __builtin_amdgcn_rcpf(p[i][1])};
  sched_fence();
  p[0] *= PAIR(0); p[1] *= PAIR(1); p[2] *= PAIR(2); p[3] *= PAIR(3);
#undef PAIR
  sched_fence();
  v4u o;
#pragma unroll
  for (int i = 0; i < 4; ++i) o[i] = pack2(p[i][0], p[i][1]);
  return o;
}
DI void split2(float y, u32& hi2, u32& lo2) {
  float r;
  asm volatile("v_cvt_pk_bf16_f32 %0, %3, %3\n\tv_and_b32 %2, 0xffff0000, %0\n\tv_sub_f32 %2, %3, %2\n\tv_cvt_pk_bf16_f32 %1, %2, %2"
               : "=&v"(hi2), "=&v"(lo2), "=&v"(r) : "v"(y));
}
DI void tile_consts(float sd, f32x2& kk) {
  float rs, k1, k2;
  asm volatile("v_rcp_f32 %0, %3\n\ts_nop 0\n\tv_mul_f32 %1, 0xc0135761, %0\n\tv_mul_f32 %2, %0, %0\n\tv_mul_f32 %2, %2, %0\n\tv_mul_f32 %2, 0xbdd2d3e8, %2"
               : "=&v"(rs), "=&v"(k1), "=&v"(k2) : "v"(sd));
  kk = f32x2{k2, k1};
}

#define STAMPX(i) do { unsigned long long t_; asm volatile("s_memrealtime %0\n\ts_waitcnt lgkmcnt(0)" : "=s"(t_) :: "memory"); stamps[i] = t_; } while (0)
#define STAMP(i) STAMPX(i)
#define FSTAMP(i)
template <int V> struct IC { static constexpr int v = V; };
#define ROT (2 * rg)
#define R7 (m ? j : 0)
#define SW (p >> 1)
#define HELP_W2 4
#define PRETOUCH 1
#define QS (rg + 3 * (p & 1))
#define DF 12
#define DO 36
#define DHF 50
#define DHO 90
#define PUBF 6
#define PRAT 28
#define WINDOW 8
#define HROT rot
}

__global__ __launch_bounds__(512, 2) void mixer_mlp(const char* x, const char* w1, const char* b1p, const char* w2, const char* b2p,
                                                    char* out, char* ws, char* ws2) {
  unsigned long long stamps[16];
  for (int i = 0; i < 16; ++i) stamps[i] = 0;
  STAMP(0);
  unsigned long long c0_ = __builtin_amdgcn_s_memtime();
  const u32 lane = threadIdx.x & 63;
  const u32 w = __builtin_amdgcn_readfirstlane(threadIdx.x >> 6) ^ 4;
  const u32 xcd = blockIdx.x & 7, idx = blockIdx.x >> 3;
  const u32 kh = xcd & 1, rg = xcd >> 1, p = idx & 3, panel = rg * 4 + p, m = idx >> 2, j = m - 1;
  const u32 rowF = panel * 131072 + (1 - kh) * 65536, rowO = panel * 131072 + kh * 65536;
  const u32 rot = ROT;
  const u32 a = w & 3;
  if (w == 0) {
    uint64_t pc;
    asm volatile("s_getpc_b64 %0" : "=s"(pc));
    dma4(mk_rsrc((const void*)(pc & ~127ull), CODE_LINES * 128), lane < 5 ? (lane * 32 + idx) * 128 : OOR, 0, L_JUNK);
  }
  u32 xv[4], wv[4];
#pragma unroll
  for (int i = 0; i < 4; ++i) {
    xv[i] = (lane >> 3) * 1024 + (((lane & 7) ^ (lane >> 4) ^ (4 * (i & 1))) * 16) + i * (8192 - 1024);
    wv[i] = (16 * a + (lane >> 4)) * (F * 2) + (((lane & 15) ^ ((lane >> 4) << 2)) * 16) + i * (4 * F * 2 - 1024);
  }
  u32 xs = (a < 2 ? rowF : rowO) + (a & 1) * 32768, w1s = kh * 1792 + j * 256, wl = a * 4096;
  asm volatile("" : "+v"(xv[0]), "+v"(xv[1]), "+v"(xv[2]), "+v"(xv[3]), "+v"(wv[0]), "+v"(wv[1]), "+v"(wv[2]), "+v"(wv[3]));
  asm volatile("" : "+s"(xs), "+s"(w1s), "+s"(wl));
  sched_fence();
  asm volatile("" : "+s"(x), "+s"(w1), "+s"(b1p), "+s"(w2), "+s"(b2p), "+s"(out), "+s"(ws), "+s"(ws2));
  const v4u rx = mk_rsrc(x, 2048 * D * 2);
  const v4u rw1 = mk_rsrc(w1, D * F * 2);
  auto dma_g = [&](u32 kg) {
    const u32 kr = (kg + rot) & 7, sl = (kg & 3) * L_SLOT + ((kg & 3) == 3 ? 16384 : 0) + wl;
    dma16x4(rx, xv, xs + kr * 128, sl);
    dma16x4(rw1, wv, w1s + kr * (64 * F * 2), sl + 16384);
  };
  FSTAMP(1);
  const v4u rws = mk_rsrc(ws, WS_BYTES);
  const u32 epo = WS_CTL + panel * 16384 + 7 * 256;
  if (w >= 4 && m != 0) { dma_g(0); dma_g(1); }
  if (w == 1) dma4_sc1(rws, lane == 0 ? 0u : OOR, epo, L_FLG);
  if (w == 2) dma4(mk_rsrc(b1p, F * 2), lane * 4, m ? w1s : 0u, L_B1);
  if (w == 3) dma4(mk_rsrc(b2p, D * 2), lane * 4, m * 128, L_B2);
  sched_fence();
  const u32 pk = panel * 2 + kh;
  const u32 g = w & 1, c = (w >> 1) & 1;
  const u32 hh = (w >> 2) ^ 1;
  const u32 t31 = lane & 31, h = lane >> 5, q4 = (lane >> 2) & 3;
  const u32 flg = WS_CTL + pk * 8192;
  const u32 sts = WS_STAT + panel * 2048;
  const u32 hpan = WS_H + pk * (4 * 56 * 1024);
  const u32 lv16 = lane * 16;
  const u32 r7 = R7, sw = SW;
  u32 jr[7];
#pragma unroll
  for (int i = 0; i < 7; ++i) jr[i] = i + r7 >= 7 ? i + r7 - 7 : i + r7;
  u32 tag, one = 1;
  pin(one);

  if (m == 0) {
    v4u xr[2][4];
    u32 ep;
    ld4_sc1(ep, rws, 0, epo);
#pragma unroll
    for (int k = 0; k < 8; ++k) ld16(xr[k >> 2][k & 3], rx, xv[0], rowO + w * 8192 + ((k + HROT) & 7) * 128);
    float sum = 0.f, sq = 0.f;
    const bf16x2 ones = {(__bf16)1.0f, (__bf16)1.0f};
    wait_vm<0>(ep);
    wait_vm4<0>(xr[0]);
    wait_vm4<0>(xr[1]);
    tag = __builtin_amdgcn_readfirstlane(ep) + 0x01010101u;
#pragma unroll
    for (int k = 0; k < 8; ++k)
#pragma unroll
      for (int e = 0; e < 4; ++e) {
        const bf16x2 v = asb2(xr[k >> 2][k & 3][e]);
        sum = __builtin_amdgcn_fdot2_f32_bf16(v, ones, sum, false);
        sq = __builtin_amdgcn_fdot2_f32_bf16(v, v, sq, false);
      }
#pragma unroll
    for (int o = 1; o < 8; o <<= 1) { sum += __shfl_xor(sum, o); sq += __shfl_xor(sq, o); }
    const float mean = sum * (1.0f / D);
    const float sd = __builtin_amdgcn_sqrtf(fmaxf(sq * (1.0f / D) - mean * mean, 0.f) + 1e-5f);
    st16_sc1<0>(v4u{asu(-mean), tag, asu(sd), tag}, rws, (lane & 7) ? OOR : (64 * kh + 8 * w + (lane >> 3)) * 16, sts);
    STAMP(1);
    bar();
  }
  if (w >= 4) {
    u32 w2v[2];
    const v4u rw2 = mk_rsrc(w2, F * D * 2);
#pragma unroll
    for (int i = 0; i < 2; ++i) w2v[i] = (16 * a + (lane >> 3)) * 1024 + (((lane & 7) ^ ((lane >> 2) & 4)) * 16) + i * (8192 - 1024);
    const u32 w2s = kh * 896 * 1024 + m * 128;
    int q = QS, jj = q + (int)r7;
    jj = jj >= 7 ? jj - 7 : jj;
    auto dma_w2 = [&](int) {
      pin_s(q); pin_s(jj);
      dma16x2(rw2, w2v, w2s + (q >= 7 ? (sw ^ 1) * 65536 : sw * 65536) + jj * 131072, q * 8192 + a * 2048);
      q = q == 13 ? 0 : q + 1;
      jj = jj == 6 ? 0 : jj + 1;
    };
    if (m != 0) {
#pragma nounroll
      for (u32 kg = 0; kg < 6; ++kg) {
        wait_vm<8>();
        bar();
        if (kg == 5) dma16_sc1(rws, lv16, sts + (a & 1) * 1024, L_ST + (a & 1) * 1024);
        dma_g(kg + 2);
      }
      wait_vm<8>();
      bar();
      tag = __builtin_amdgcn_readfirstlane(*(const LAS u32*)L_FLG) + 0x01010101u;
    }
    else {
      const u32 pv[4] = {lv16, lv16, lv16, lv16};
#pragma nounroll
      for (u32 i = 0; i < HELP_W2; ++i) {
        u32 rb = rg * 14 + i * 4 + a;
        rb = rb >= 56 ? rb - 56 : rb;
        dma16x4(rw2, pv, kh * 896 * 1024 + (p * 56 + rb) * 4096, L_RED + a * 4096);
      }
    }
    dma_w2(0); dma_w2(1);
    if (m != 0) {
      wait_vm<4>();
      bar();
      if (m == 1 && w == 4) st4_sc1(tag, rws, lane == 0 ? 0u : OOR, flg + 16 * 256);
    }
    dma_w2(2); dma_w2(3); dma_w2(4); dma_w2(5); dma_w2(6); dma_w2(7);
    dma_w2(8); dma_w2(9); dma_w2(10); dma_w2(11); dma_w2(12); dma_w2(13);
    u32 old = 0;
    wait_vm<0>();
    lds_inc_lane0(old, L_FLG + 80, one);
    asm volatile("s_waitcnt lgkmcnt(0)" : "+v"(old)::"memory");
    STAMP(6);
    __builtin_amdgcn_s_setprio(2);
  } else {
    if (m != 0) {
      if (w == 0) {
        const u32 tv = lane == 0 ? hpan : lane == 1 ? hpan + 4 * 56 * 1024 - 128 : lane == 2 ? WS_P + (pk * 32 + m * 4) * 4096 :
                       lane == 3 ? WS_P + ((pk ^ 1) * 32 + m * 4) * 4096 : (PRETOUCH > 1 && lane < 6) ? flg + (lane - 4) * 2048 + j * 256 : OOR;
        dma4(rws, tv, 0, L_JUNK);
        dma4(mk_rsrc(out, 2048 * D * 2), lane == 0 ? rowO : OOR, 0, L_JUNK);
      }
      u32 xa[2][4], wa[2];
#pragma unroll
      for (int k = 0; k < 4; ++k) {
        xa[0][k] = t31 * 128 + (((2 * k + h) ^ ((t31 >> 1) & 7)) * 16);
        xa[1][k] = xa[0][k] + 2 * L_SLOT;
        pin(xa[0][k]); pin(xa[1][k]);
      }
      wa[0] = 16384 + (8 * h + q4) * 256 + ((w ^ q4) * 64) + ((lane >> 4) & 1) * 32 + (lane & 3) * 8;
      wa[1] = wa[0] + 2 * L_SLOT;
      pin(wa[0]); pin(wa[1]);
      const u32 selw = ((lane & 3) == 0 && ((lane >> 2) & 1) == ((lane >> 4) & 1)) ? 0x3f803f80u : 0u;
      v4u selv = {selw, selw, selw, selw};
      pin(selv);
      const bf16x8 sel = __builtin_bit_cast(bf16x8, selv);
      u32 sta = L_ST + t31 * 16, b1a = L_B1 + w * 64 + t31 * 2;
      pin(sta); pin(b1a);
      bf16x8 xf[4][4], wf[4];
      f32x16 acc[4];
      f32x4 cs;
      v4u st[4], bfr[4];
      f32x2 kk[4], sd[4];
      u32 ep, b1w;
      auto rd = [&](auto N, auto PART) {
        constexpr int kg = N.v >> 2, k = N.v & 3;
        constexpr u32 sl = (kg & 1) * L_SLOT + ((kg & 3) == 3 ? 16384 : 0);
        if (PART.v == 0) wf[k] = lds_tr(wa[(kg >> 1) & 1] + sl + k * 4096, wa[(kg >> 1) & 1] + sl + k * 4096 + 1024);
        else {
          xf[k][2 * PART.v - 2] = lds_b128(xa[(kg >> 1) & 1][k] + sl + (2 * PART.v - 2) * 4096);
          xf[k][2 * PART.v - 1] = lds_b128(xa[(kg >> 1) & 1][k] + sl + (2 * PART.v - 1) * 4096);
        }
      };
      auto prep = [&](auto T) {
        const u32 nm = st[T.v].x, sdv = st[T.v].z;
        u32 hi2, lo2;
        split2(asf(h ? sdv : nm), hi2, lo2);
        const u32 y = (hi2 & 0xffffu) | (lo2 & 0xffff0000u);
        bfr[T.v] = v4u{y, y, 0u, 0u};
        tile_consts(asf(sdv), kk[T.v]);
        sd[T.v] = f32x2{asf(sdv), asf(sdv)};
        pin(bfr[T.v]); pin(sd[T.v]); pin(kk[T.v]);
      };
      auto step = [&](auto N) {
        constexpr int n = N.v, mmn = n - 2, k = mmn & 3;
        if constexpr (mmn >= 0) mfma32<mmn == 0>(acc[0], wf[k], xf[k][0]);
        sched_fence();
        if constexpr (n < 32) rd(N, IC<0>{});
        sched_fence();
        if constexpr (mmn >= 0) mfma32<mmn == 0>(acc[1], wf[k], xf[k][1]);
        sched_fence();
        if constexpr (n < 32) rd(N, IC<1>{});
        sched_fence();
        if constexpr (mmn >= 0) mfma32<mmn == 0>(acc[2], wf[k], xf[k][2]);
        sched_fence();
        if constexpr (n < 32) rd(N, IC<2>{});
        sched_fence();
        if constexpr (mmn >= 0) {
          mfma32<mmn == 0>(acc[3], wf[k], xf[k][3]);
          sched_fence();
          if constexpr (n >= 28 && n < 32) prep(IC<n - 28>{});
          sched_fence();
          if constexpr (mmn < 30) mfma16<mmn == 0>(cs, sel, wf[k]);
        }
        sched_fence();
      };
      auto iter = [&](auto KG) {
        constexpr int kg = KG.v;
        if (kg == 2) wait_vm<0>();
        wait_lgkm0();
        bar();
        if (kg == 0) STAMP(1);
        FSTAMP(2 + kg);
        step(IC<4 * kg>{});
        if constexpr (kg == 3) { ep = *(const LAS u32*)L_FLG; b1w = *(const LAS unsigned short*)b1a; sched_fence(); }
        if constexpr (kg == 6) {
#pragma unroll
          for (int t = 0; t < 4; ++t) st[t] = *(const LAS v4u*)(sta + (((t >> 1) ^ kh ^ 1) * 2 + (t & 1)) * 512);
          sched_fence();
        }
        step(IC<4 * kg + 1>{}); step(IC<4 * kg + 2>{}); step(IC<4 * kg + 3>{});
        if constexpr (kg == 6) {
          tag = __builtin_amdgcn_readfirstlane(ep) + 0x01010101u;
#pragma nounroll
          for (u32 spin = 0; spin < SPIN_CAP; ++spin) {
            u32 bad = 0;
#pragma unroll
            for (int t = 0; t < 4; ++t) bad |= (st[t].y ^ tag) | (st[t].w ^ tag);
            if (__builtin_amdgcn_ballot_w64(bad != 0) == 0) break;
#pragma unroll
            for (int t = 0; t < 4; ++t) ld16_sc1_w(st[t], rws, t31 * 16, sts + (((t >> 1) ^ kh ^ 1) * 2 + (t & 1)) * 512);
          }
        }
      };
      iter(IC<0>{}); iter(IC<1>{}); iter(IC<2>{}); iter(IC<3>{}); iter(IC<4>{}); iter(IC<5>{}); iter(IC<6>{}); iter(IC<7>{});
      FSTAMP(10);
      mfma16<false>(cs, sel, wf[2]);
      mfma16<false>(cs, sel, wf[3]);
      step(IC<32>{});
      asm volatile("s_nop 3" : "+v"(cs));
      v4u afr;
      {
        u32 hi2, lo2;
        split2(h ? bf_lo(b1w) : cs[0], hi2, lo2);
        afr = v4u{hi2, lo2, 0u, 0u};
        pin(afr);
      }
      sched_fence();
      step(IC<33>{});
#pragma unroll
      for (int t = 0; t < 4; ++t) mfma32<false>(acc[t], __builtin_bit_cast(bf16x8, afr), __builtin_bit_cast(bf16x8, bfr[t]));
      __builtin_amdgcn_s_setprio(3);
      const u32 hdst = hpan + (w >> 1) * (28 * 1024) + j * 4096 + (w & 1) * 2048;
      u32 tagv = tag, cnt = L_FLG + 64;
      pin(tagv); pin(cnt);
      asm volatile("s_nop 8" : "+v"(acc[0]), "+v"(acc[1]), "+v"(acc[2]), "+v"(acc[3]));
      STAMP(2);
      FSTAMP(11);
      auto signal = [&](u32& old, u32 stepi) {
        asm volatile("s_waitcnt lgkmcnt(0)" ::: "memory");
        sched_fence();
        pin(old);
        st4_sc1(tagv, rws, __builtin_amdgcn_readfirstlane(old) == 3 && lane < 32 ? lane * 4 : OOR, flg + stepi * 2048 + j * 256);
      };
      auto gfrag = [&](auto N) {
        constexpr int t = N.v >> 1, s = N.v & 1;
        v4u o = gelu8<s>(acc[t], kk[t], sd[t]);
        u32 old = 0;
        pin(o);
        if (N.v == PUBF) { wait_vm<PUBF - 4>(); lds_inc_lane0(old, cnt, one); signal(old, 0); }
        st16_sc1<s * 1024>(o, rws, lv16, hdst + t * (56 * 1024));
      };
      gfrag(IC<0>{}); gfrag(IC<1>{}); gfrag(IC<2>{}); gfrag(IC<3>{});
      STAMP(3);
      gfrag(IC<4>{}); gfrag(IC<5>{}); gfrag(IC<6>{});
      STAMP(4);
      gfrag(IC<7>{});
      STAMP(5);
      u32 old = 0;
      wait_vm<0>();
      lds_inc_lane0(old, cnt + 4, one);
      signal(old, 1);
      __builtin_amdgcn_s_setprio(0);
      STAMP(6);
    }
  }

  {
    const u32 pollv = lane < 7 ? lane * 256 : OOR, polls = flg + hh * 2048;
    const u32 hsrc = hpan + (hh * 2 + g) * (56 * 1024) + c * (28 * 1024);
    const u32 p4 = lane & 3, blk = (lane >> 4) & 1;
    u32 a0 = (c ^ sw) * 57344 + (4 * h + q4) * 128 + (((q4 >> 1) ^ c) * 64) + (4 * (p4 & 1) + 2 * blk + (p4 >> 1)) * 8;
    u32 a1 = a0 ^ 64;
    pin(a0); pin(a1);
    u32 fv;
    if (m == 0) {
      fv = ~tag;
      poll_eq<1, 4>(fv, rws, 0, flg + 16 * 256, tag);
      if (hh) __builtin_amdgcn_s_sleep(DHO - DHF);
      __builtin_amdgcn_s_sleep(DHF - POLL_SLEEP);
      fv = ~tag;
      poll_eq<127, POLL_SLEEP>(fv, rws, pollv, polls, tag);
    } else {
      lds_wait_eq<1>(L_FLG + 64 + hh * 4, 4);
    }
    STAMP(7);
    v4u hf[28], pr[4];
    auto ld_h = [&](auto I) { ld16_sc1<(I.v & 3) * 1024>(hf[I.v], rws, lv16, hsrc + jr[I.v >> 2] * 4096); };
    constexpr int WIN = WINDOW;
    ld_h(IC<0>{}); ld_h(IC<1>{}); ld_h(IC<2>{}); ld_h(IC<3>{});
    f32x16 acc0, acc1;
    bf16x8 wa0[3], wa1[3];
    auto rd_w = [&](auto I, auto N) {
      if (N.v) wa1[I.v % 3] = lds_tr(a1 + I.v * 2048, a1 + I.v * 2048 + 1024);
      else wa0[I.v % 3] = lds_tr(a0 + I.v * 2048, a0 + I.v * 2048 + 1024);
    };
    lds_wait_eq<0>(L_FLG + 80, 4);
    rd_w(IC<0>{}, IC<1>{}); rd_w(IC<0>{}, IC<0>{}); rd_w(IC<1>{}, IC<1>{}); rd_w(IC<1>{}, IC<0>{});
    if (m != 0) { if (hh) __builtin_amdgcn_s_sleep(DO); else __builtin_amdgcn_s_sleep(DF); }
    ld4_sc1(fv, rws, pollv, polls);
    const u32 pslot = m * 4 + g * 2 + c;
    const u32 psrc = WS_P + ((pk ^ 1) * 32 + pslot) * 4096;
    auto g2 = [&](auto I) {
      constexpr int i = I.v;
      constexpr int younger = i < 4 ? 4 - i : i + WIN < 28 ? WIN - 1 : 27 - i;
      wait_vm<younger>(hf[i]);
      mfma32<i == 0>(acc1, wa1[i % 3], __builtin_bit_cast(bf16x8, hf[i]));
      sched_fence();
      if constexpr (i >= 4 && i + WIN < 28) ld_h(IC<i + WIN>{});
      if constexpr (i + 2 < 28) rd_w(IC<i + 2>{}, IC<1>{});
      sched_fence();
      mfma32<i == 0>(acc0, wa0[i % 3], __builtin_bit_cast(bf16x8, hf[i]));
      sched_fence();
      if constexpr (i + 2 < 28) rd_w(IC<i + 2>{}, IC<0>{});
      sched_fence();
    };
    g2(IC<0>{}); g2(IC<1>{}); g2(IC<2>{}); g2(IC<3>{});
    wait_vm<0>(fv);
    poll_eq<127, POLL_SLEEP>(fv, rws, pollv, polls, tag);
    STAMP(8);
    ld_h(IC<4>{}); ld_h(IC<5>{}); ld_h(IC<6>{}); ld_h(IC<7>{}); ld_h(IC<8>{}); ld_h(IC<9>{});
    if constexpr (WIN > 6) { ld_h(IC<10>{}); ld_h(IC<11>{}); }
    static_assert(WIN == 6 || WIN == 8, "");
    g2(IC<4>{}); g2(IC<5>{}); g2(IC<6>{});
    g2(IC<7>{}); g2(IC<8>{}); g2(IC<9>{}); g2(IC<10>{}); g2(IC<11>{}); g2(IC<12>{}); g2(IC<13>{});
    g2(IC<14>{}); g2(IC<15>{}); g2(IC<16>{}); g2(IC<17>{}); g2(IC<18>{}); g2(IC<19>{}); g2(IC<20>{});
    g2(IC<21>{}); g2(IC<22>{}); g2(IC<23>{}); g2(IC<24>{}); g2(IC<25>{}); g2(IC<26>{}); g2(IC<27>{});
    STAMP(9);
    const u32 red = L_RED + (w ^ 2) * 4096;
    auto exchange = [&]() {
      nop12(acc0, acc1);
#pragma unroll
      for (int q = 0; q < 4; ++q)
        *(LAS f32x4*)(L_RED + w * 4096 + q * 1024 + lv16) = f32x4{acc1[4 * q], acc1[4 * q + 1], acc1[4 * q + 2], acc1[4 * q + 3]};
      wait_lgkm0();
      *(volatile LAS u32*)(L_FLG + 32 + w * 4) = 1;
      lds_wait_eq<0>(L_FLG + 32 + (w ^ 2) * 4, 1);
#pragma unroll
      for (int q = 0; q < 4; ++q) {
        f32x4 o = lds_f4(red + q * 1024 + lv16);
#pragma unroll
        for (int e = 0; e < 4; ++e) acc0[4 * q + e] += o[e];
      }
    };
    if (hh) { ld16_sc1<0>(pr[0], rws, lv16, psrc); ld16_sc1<1024>(pr[1], rws, lv16, psrc); ld16_sc1<2048>(pr[2], rws, lv16, psrc); ld16_sc1<3072>(pr[3], rws, lv16, psrc); }
    exchange();
    STAMP(10);
    if (hh == 0) {
      const u32 pdst = WS_P + (pk * 32 + pslot) * 4096;
      v4u d[4];
#pragma unroll
      for (int q = 0; q < 4; ++q)
#pragma unroll
        for (int e = 0; e < 4; ++e) d[q][e] = (asu(acc0[4 * q + e]) & 0xffffff00u) | ((tag >> (8 * e)) & 0xffu);
      st16_sc1<0>(d[0], rws, lv16, pdst); st16_sc1<1024>(d[1], rws, lv16, pdst); st16_sc1<2048>(d[2], rws, lv16, pdst); st16_sc1<3072>(d[3], rws, lv16, pdst);
      STAMP(11);
    } else {
      const v4u bb0 = *(const LAS v4u*)(L_B2 + c * 64 + h * 32), bb1 = *(const LAS v4u*)(L_B2 + c * 64 + h * 32 + 16);
      wait_vm4<0>(pr);
#pragma nounroll
      for (u32 spin = 0;; ++spin) {
        u32 bad = 0;
#pragma unroll
        for (int q = 0; q < 4; ++q)
#pragma unroll
          for (int e = 0; e < 4; ++e) bad |= pr[q][e] ^ (tag >> (8 * e));
        if (__builtin_amdgcn_ballot_w64((bad & 0xffu) != 0) == 0 || spin >= SPIN_CAP) break;
        __builtin_amdgcn_s_sleep(POLL_SLEEP);
        ld64_sc1_w(pr, rws, lv16, psrc);
      }
      STAMP(11);
      v4u o0, o1;
#pragma unroll
      for (int e = 0; e < 4; ++e) {
        o0[e] = pack2(acc0[2 * e] + asf(pr[e >> 1][(2 * e) & 3] & 0xffffff00u) + bf_lo(bb0[e]),
                      acc0[2 * e + 1] + asf(pr[e >> 1][(2 * e + 1) & 3] & 0xffffff00u) + bf_hi(bb0[e]));
        o1[e] = pack2(acc0[8 + 2 * e] + asf(pr[2 + (e >> 1)][(2 * e) & 3] & 0xffffff00u) + bf_lo(bb1[e]),
                      acc0[8 + 2 * e + 1] + asf(pr[2 + (e >> 1)][(2 * e + 1) & 3] & 0xffffff00u) + bf_hi(bb1[e]));
      }
      const u32 ta = red + t31 * 64 + (((2 * h) ^ ((t31 >> 1) & 3)) * 16);
      *(LAS v4u*)ta = o0;
      *(LAS v4u*)(ta ^ 16) = o1;
      const u32 tb = red + (lane >> 2) * 64 + (((lane & 3) ^ ((lane >> 3) & 3)) * 16);
      o0 = *(const LAS v4u*)tb;
      o1 = *(const LAS v4u*)(tb + 1024);
      const v4u ro = mk_rsrc(out, 2048 * D * 2);
      const u32 ov = (32 * g + (lane >> 2)) * 1024 + (lane & 3) * 16;
      const u32 os = rowO + m * 128 + c * 64;
      st16_out(o0, ro, ov, os);
      st16_out(o1, ro, ov, os + 16 * 1024);
      if (kh == 0 && m == 0 && w == 0) st4_sc1(tag, rws, lane == 0 ? 0u : OOR, epo);
    }
    STAMP(12);
  }
  wait_vm<0>();
  STAMP(14);
  stamps[15] = stamps[0] + (__builtin_amdgcn_s_memtime() - c0_) * 100 / (stamps[14] - stamps[0]);
  if (stamps[1] != 0 && blockIdx.x == 77 && w == 0) { unsigned long long until = stamps[0] + 5000 + 10 * (stamps[1] - stamps[0]); while (__builtin_amdgcn_s_memrealtime() < until) __builtin_amdgcn_s_sleep(1); }
}

extern "C" void kernel_launch(void* const* d_in, const int* in_sizes, int n_in, void* d_out, int out_size, void* d_ws,
                              size_t ws_size, hipStream_t stream) {
  (void)in_sizes; (void)n_in; (void)out_size; (void)ws_size;
  (void)hipFuncSetAttribute((const void*)mixer_mlp, hipFuncAttributeMaxDynamicSharedMemorySize, L_BYTES);
  mixer_mlp<<<256, 512, L_BYTES, stream>>>((const char*)d_in[0], (const char*)d_in[1], (const char*)d_in[2], (const char*)d_in[3],
                                           (const char*)d_in[4], (char*)d_out, (char*)d_ws, (char*)d_ws);
}
```

```cpp
#include <hip/hip_runtime.h>
#include <stdint.h>

typedef unsigned u32;
typedef unsigned v2u __attribute__((ext_vector_type(2)));
typedef unsigned v4u __attribute__((ext_vector_type(4)));
typedef short s16x4 __attribute__((ext_vector_type(4)));
typedef __bf16 bf16x2 __attribute__((ext_vector_type(2)));
typedef __bf16 bf16x8 __attribute__((ext_vector_type(8)));
typedef float f32x2 __attribute__((ext_vector_type(2)));
typedef float f32x4 __attribute__((ext_vector_type(4)));
typedef float f32x16 __attribute__((ext_vector_type(16)));
#define LAS __attribute__((address_space(3)))
#define DI __device__ __forceinline__

namespace {
constexpr int D = 512, F = 1792;
constexpr u32 WS_CTL = 0;
constexpr u32 WS_STAT = 1u << 18;
constexpr u32 WS_H = 1u << 20;
constexpr u32 H_STRIDE = 4 * 56 * 1024 + 256;
constexpr u32 WS_P = 9u << 20;
constexpr u32 P_STRIDE = 8192 + 256;
constexpr u32 WS_BYTES = 12u << 20;
constexpr u32 L_SLOT = 32768;
constexpr u32 L_RED = 114688;
constexpr u32 L_ST = 155648;
constexpr u32 L_B1 = L_ST + 2048;
constexpr u32 L_B2 = L_B1 + 256;
constexpr u32 L_FLG = L_B2 + 256;
constexpr u32 L_JUNK = L_FLG + 256;
constexpr u32 L_BYTES = 163840;
constexpr u32 CODE_LINES = 133;
constexpr u32 SPIN_CAP = 1u << 14;
constexpr u32 OOR = 0x7ffffff0u;

constexpr u32 slot_of(int kg) { return kg % 3 == 0 ? 0 : kg % 3 == 1 ? L_RED : L_SLOT; }
DI v4u mk_rsrc(const void* p, u32 bytes) { return v4u{(u32)(uint64_t)p, (u32)((uint64_t)p >> 32) & 0xffffu, bytes, 0x00020000u}; }
DI void dma16x4(v4u rs, const u32 (&v)[4], u32 soff, u32 lds) {
  asm volatile("s_mov_b32 m0, %6\n\ts_nop 0\n\tbuffer_load_dwordx4 %0, %4, %5 offen lds\n\tbuffer_load_dwordx4 %1, %4, %5 offen offset:1024 lds\n\t"
               "buffer_load_dwordx4 %2, %4, %5 offen offset:2048 lds\n\tbuffer_load_dwordx4 %3, %4, %5 offen offset:3072 lds"
               ::"v"(v[0]), "v"(v[1]), "v"(v[2]), "v"(v[3]), "s"(rs), "s"(soff), "s"(lds) : "memory");
}
DI void dma16x2(v4u rs, const u32 (&v)[2], u32 soff, u32 lds) {
  asm volatile("s_mov_b32 m0, %4\n\ts_nop 0\n\tbuffer_load_dwordx4 %0, %2, %3 offen lds\n\tbuffer_load_dwordx4 %1, %2, %3 offen offset:1024 lds"
               ::"v"(v[0]), "v"(v[1]), "s"(rs), "s"(soff), "s"(lds) : "memory");
}
DI void dma4(v4u rs, u32 voff, u32 soff, u32 lds) {
  asm volatile("s_mov_b32 m0, %3\n\ts_nop 0\n\tbuffer_load_dword %0, %1, %2 offen lds" ::"v"(voff), "s"(rs), "s"(soff), "s"(lds) : "memory");
}
DI void dma4_sc1(v4u rs, u32 voff, u32 soff, u32 lds) {
  asm volatile("s_mov_b32 m0, %3\n\ts_nop 0\n\tbuffer_load_dword %0, %1, %2 offen sc1 lds" ::"v"(voff), "s"(rs), "s"(soff), "s"(lds) : "memory");
}
DI void dma16_sc1(v4u rs, u32 voff, u32 soff, u32 lds) {
  asm volatile("s_mov_b32 m0, %3\n\ts_nop 0\n\tbuffer_load_dwordx4 %0, %1, %2 offen sc1 lds" ::"v"(voff), "s"(rs), "s"(soff), "s"(lds) : "memory");
}
template <int OFF> DI void ld16_sc1(v4u& d, v4u rs, u32 voff, u32 soff) {
  asm volatile("buffer_load_dwordx4 %0, %1, %2, %3 offen offset:%4 sc1" : "=v"(d) : "v"(voff), "s"(rs), "s"(soff), "i"(OFF) : "memory");
}
DI void ld16(v4u& d, v4u rs, u32 voff, u32 soff) {
  asm volatile("buffer_load_dwordx4 %0, %1, %2, %3 offen" : "=v"(d) : "v"(voff), "s"(rs), "s"(soff) : "memory");
}
DI void ld4_sc1(u32& d, v4u rs, u32 voff, u32 soff) {
  asm volatile("buffer_load_dword %0, %1, %2, %3 offen sc1" : "=v"(d) : "v"(voff), "s"(rs), "s"(soff) : "memory");
}
DI void ld16_sc1_w(v4u& d, v4u rs, u32 voff, u32 soff) {
  asm volatile("buffer_load_dwordx4 %0, %1, %2, %3 offen sc1\n\ts_waitcnt vmcnt(0)" : "=&v"(d) : "v"(voff), "s"(rs), "s"(soff) : "memory");
}
DI void ld4_sc1_w(u32& d, v4u rs, u32 voff, u32 soff) {
  asm volatile("buffer_load_dword %0, %1, %2, %3 offen sc1\n\ts_waitcnt vmcnt(0)" : "=&v"(d) : "v"(voff), "s"(rs), "s"(soff) : "memory");
}
DI void ld32_sc1_w(v4u (&d)[2], v4u rs, u32 voff, u32 soff) {
  asm volatile("buffer_load_dwordx4 %0, %2, %3, %4 offen sc1\n\tbuffer_load_dwordx4 %1, %2, %3, %4 offen offset:1024 sc1\n\ts_waitcnt vmcnt(0)"
               : "=&v"(d[0]), "=&v"(d[1]) : "v"(voff), "s"(rs), "s"(soff) : "memory");
}
template <int OFF> DI void st16_sc1(v4u d, v4u rs, u32 voff, u32 soff) {
  asm volatile("buffer_store_dwordx4 %0, %1, %2, %3 offen offset:%4 sc1\n\ts_nop 1" ::"v"(d), "v"(voff), "s"(rs), "s"(soff), "i"(OFF) : "memory");
}
#define OUT_FLAGS " sc0 sc1"
#define POLL_SLEEP 6
DI void st16_out(v4u d, v4u rs, u32 voff, u32 soff) {
  asm volatile("buffer_store_dwordx4 %0, %1, %2, %3 offen" OUT_FLAGS "\n\ts_nop 1" ::"v"(d), "v"(voff), "s"(rs), "s"(soff) : "memory");
}
DI void st4_sc1(u32 d, v4u rs, u32 voff, u32 soff) {
  asm volatile("buffer_store_dword %0, %1, %2, %3 offen sc1" ::"v"(d), "v"(voff), "s"(rs), "s"(soff) : "memory");
}
template <int N> DI void wait_vm() { asm volatile("s_waitcnt vmcnt(%0)" ::"i"(N) : "memory"); }
template <int N, class T> DI void wait_vm(T& r) {
  wait_vm<N>();
  __builtin_amdgcn_sched_barrier(0);
  asm volatile("" : "+v"(r));
}
template <int N> DI void wait_vm4(v4u (&r)[4]) {
  wait_vm<N>();
  __builtin_amdgcn_sched_barrier(0);
  asm volatile("" : "+v"(r[0]), "+v"(r[1]), "+v"(r[2]), "+v"(r[3]));
}
DI void lds_inc_lane0(u32& old, u32 addr, u32 one) {
  asm volatile("s_mov_b64 exec, 1\n\tds_add_rtn_u32 %0, %1, %2\n\ts_mov_b64 exec, -1" : "+v"(old) : "v"(addr), "v"(one) : "memory");
}
DI void wait_lgkm0() { __builtin_amdgcn_s_waitcnt(0xC07F); asm volatile("" ::: "memory"); }
DI void bar() { asm volatile("s_barrier" ::: "memory"); }
DI void sched_fence() { __builtin_amdgcn_sched_barrier(0); }
template <class T> DI void pin(T& v) { asm volatile("" : "+v"(v)); }

DI void lgkm0_pin(u32& v) { asm volatile("s_waitcnt lgkmcnt(0)" : "+v"(v)::"memory"); }
DI void pin_s(int& v) { asm volatile("" : "+s"(v)); }
template <int SLEEP, int VAL> DI void lds_wait(u32 addr) {
  u32 t, n;
  asm volatile("s_mov_b32 %1, %5\n1:\n\tds_read_b32 %0, %2\n\ts_waitcnt lgkmcnt(0)\n\tv_cmp_ne_u32 vcc, %3, %0\n\ts_nop 0\n\ts_cmp_eq_u64 vcc, 0\n\t"
               "s_cbranch_scc1 2f\n\ts_sleep %4\n\ts_sub_u32 %1, %1, 1\n\ts_cbranch_scc0 1b\n2:"
               : "=&v"(t), "=&s"(n) : "v"(addr), "i"(VAL), "i"(SLEEP), "i"(SPIN_CAP << 4) : "memory", "scc", "vcc");
}
template <u32 MASK, int SLEEP> DI void poll_eq(u32& f, v4u rs, u32 voff, u32 soff, u32 tag) {
  u32 n;
  asm volatile("s_mov_b32 %1, %8\n1:\n\tv_cmp_ne_u32 vcc, %5, %0\n\ts_nop 0\n\ts_and_b32 vcc_lo, vcc_lo, %6\n\ts_cbranch_scc0 2f\n\ts_sleep %7\n\t"
               "buffer_load_dword %0, %2, %3, %4 offen sc1\n\ts_sub_u32 %1, %1, 1\n\ts_waitcnt vmcnt(0)\n\ts_cbranch_scc0 1b\n2:"
               : "+v"(f), "=&s"(n) : "v"(voff), "s"(rs), "s"(soff), "s"(tag), "i"(MASK), "i"(SLEEP), "i"(SPIN_CAP) : "memory", "scc", "vcc");
}
DI bf16x8 lds_b128(u32 a) { return *(const LAS bf16x8*)a; }
DI f32x4 lds_f4(u32 a) { return *(const LAS f32x4*)a; }
DI bf16x8 lds_tr(u32 a0, u32 a1) {
  s16x4 lo = __builtin_amdgcn_ds_read_tr16_b64_v4i16((LAS s16x4*)a0);
  s16x4 hi = __builtin_amdgcn_ds_read_tr16_b64_v4i16((LAS s16x4*)a1);
  return __builtin_bit_cast(bf16x8, __builtin_shufflevector(lo, hi, 0, 1, 2, 3, 4, 5, 6, 7));
}
template <bool FIRST> DI void mfma32(f32x16& c, bf16x8 a, bf16x8 b) {
  if (FIRST) asm volatile("v_mfma_f32_32x32x16_bf16 %0, %1, %2, 0" : "=&v"(c) : "v"(a), "v"(b));
  else asm volatile("v_mfma_f32_32x32x16_bf16 %0, %1, %2, %0" : "+v"(c) : "v"(a), "v"(b));
}
template <bool FIRST> DI void mfma16(f32x4& c, bf16x8 a, bf16x8 b) {
  if (FIRST) asm volatile("v_mfma_f32_16x16x32_bf16 %0, %1, %2, 0" : "=&v"(c) : "v"(a), "v"(b));
  else asm volatile("v_mfma_f32_16x16x32_bf16 %0, %1, %2, %0" : "+v"(c) : "v"(a), "v"(b));
}
DI void nop12(f32x16& a, f32x16& b) { asm volatile("s_nop 11" : "+v"(a), "+v"(b)); }
DI u32 pack2(float a, float b) { bf16x2 v = {(__bf16)a, (__bf16)b}; return __builtin_bit_cast(u32, v); }
DI float asf(u32 w) { return __builtin_bit_cast(float, w); }
DI u32 asu(float f) { return __builtin_bit_cast(u32, f); }
DI bf16x2 asb2(u32 w) { return __builtin_bit_cast(bf16x2, w); }
DI float bf_lo(u32 w) { return asf(w << 16); }
DI float bf_hi(u32 w) { return asf(w & 0xffff0000u); }

template <int S> DI v4u gelu8(const f32x16& acc, f32x2 kk, f32x2 sd) {
  f32x2 p[4];
#define PAIR(i) __builtin_shufflevector(acc, acc, 8 * S + 2 * i, 8 * S + 2 * i + 1)
  asm volatile(
      "v_pk_mul_f32 %0, %5, %5\n\tv_pk_mul_f32 %1, %6, %6\n\tv_pk_mul_f32 %2, %7, %7\n\tv_pk_mul_f32 %3, %8, %8\n\t"
      "v_pk_fma_f32 %0, %0, %4, %4 op_sel:[0,0,1] op_sel_hi:[1,0,1]\n\tv_pk_fma_f32 %1, %1, %4, %4 op_sel:[0,0,1] op_sel_hi:[1,0,1]\n\t"
      "v_pk_fma_f32 %2, %2, %4, %4 op_sel:[0,0,1] op_sel_hi:[1,0,1]\n\tv_pk_fma_f32 %3, %3, %4, %4 op_sel:[0,0,1] op_sel_hi:[1,0,1]\n\t"
      "v_pk_mul_f32 %0, %0, %5\n\tv_pk_mul_f32 %1, %1, %6\n\tv_pk_mul_f32 %2, %2, %7\n\tv_pk_mul_f32 %3, %3, %8"
      : "=&v"(p[0]), "=&v"(p[1]), "=&v"(p[2]), "=&v"(p[3])
      : "v"(kk), "v"(PAIR(0)), "v"(PAIR(1)), "v"(PAIR(2)), "v"(PAIR(3)));
#pragma unroll
  for (int i = 0; i < 4; ++i) p[i] = f32x2{__builtin_amdgcn_exp2f(p[i][0]), __builtin_amdgcn_exp2f(p[i][1])};
  sched_fence();
#pragma unroll
  for (int i = 0; i < 4; ++i) p[i] = __builtin_elementwise_fma(p[i], sd, sd);
  sched_fence();
#pragma unroll
  for (int i = 0; i < 4; ++i) p[i] = f32x2{__builtin_amdgcn_rcpf(p[i][0]), __builtin_amdgcn_rcpf(p[i][1])};
  sched_fence();
  p[0] *= PAIR(0); p[1] *= PAIR(1); p[2] *= PAIR(2); p[3] *= PAIR(3);
#undef PAIR
  sched_fence();
  v4u o;
#pragma unroll
  for (int i = 0; i < 4; ++i) o[i] = pack2(p[i][0], p[i][1]);
  return o;
}
DI void split2(float y, u32& hi2, u32& lo2) {
  float r;
  asm volatile("v_cvt_pk_bf16_f32 %0, %3, %3\n\tv_and_b32 %2, 0xffff0000, %0\n\tv_sub_f32 %2, %3, %2\n\tv_cvt_pk_bf16_f32 %1, %2, %2"
               : "=&v"(hi2), "=&v"(lo2), "=&v"(r) : "v"(y));
}
DI void tile_consts(float rs, f32x2& kk) {
  float k1, k2;
  asm volatile("v_mul_f32 %1, %2, %2\n\tv_mul_f32 %0, 0xc0135761, %2\n\tv_mul_f32 %1, %1, %2\n\tv_mul_f32 %1, 0xbdd2d3e8, %1"
               : "=&v"(k1), "=&v"(k2) : "v"(rs));
  kk = f32x2{k2, k1};
}
DI f32x4 add4(f32x4 a, f32x4 o) {
  float r[4];
  asm volatile("v_add_f32 %0, %4, %8\n\tv_add_f32 %1, %5, %9\n\tv_add_f32 %2, %6, %10\n\tv_add_f32 %3, %7, %11"
               : "=&v"(r[0]), "=&v"(r[1]), "=&v"(r[2]), "=v"(r[3])
               : "v"(a[0]), "v"(a[1]), "v"(a[2]), "v"(a[3]), "v"(o[0]), "v"(o[1]), "v"(o[2]), "v"(o[3]));
  return f32x4{r[0], r[1], r[2], r[3]};
}
DI void mfma32k8(f32x16& c, v2u a, v2u b) { asm volatile("v_mfma_f32_32x32x8_bf16 %0, %1, %2, %0" : "+v"(c) : "v"(a), "v"(b)); }

#define STAMP(i)
#define FSTAMP(i)
#define GSTAMP(i)
template <int V> struct IC { static constexpr int v = V; };
#define ROT ((rg + 4 * (p & 1) + 2 * kh) & 7)
#define R7 (m ? j : 0)
#define SW (p >> 1)
#define HELP_W2 4
#define GOAT 17
#define PAD1 -1
#define PAD2 -1
#define RP (rg + 3 * (p & 1))
#define RELAT -1
#define DP 0
#define WSL 1
#define AB_SAMEBLK 0
#define AB_NORD 0
#define AB_NOLD 0
#define ARR 0
#define MISC 1
#define TOUCH 2
#define CLRH 0
#define CLR_AT 3
#define WPAD 0
#define AB_NOCS 0
#define KS0 0
#define KS1 8
#define KSLEEP 3
#define EARLY0 4
#define N6 4
#define DF 11
#define DO 12
#define DHF 22
#define DHO 62
#define PUBF 4
#define PRAT 28
#define WINDOW 10
#define HROT (rot + 1)
}

__global__ __launch_bounds__(512, 2) void mixer_mlp(const char* x, const char* w1, const char* b1p, const char* w2, const char* b2p,
                                                    char* out, char* ws, char* ws2) {
  {
    uint64_t pc;
    u32 o, so;
    asm volatile("s_getpc_b64 %0\n\ts_lshl_b32 %2, %4, 4\n\tv_lshl_add_u32 %1, %3, 12, %2\n\tv_min_u32 %1, %5, %1\n\ts_mov_b32 m0, %6\n\ts_nop 0\n\t"
                 "global_load_lds_dword %1, %0"
                 : "=&s"(pc), "=&v"(o), "=&s"(so) : "v"(threadIdx.x), "s"(blockIdx.x), "i"(CODE_LINES * 128 - 128), "i"(L_JUNK) : "memory", "scc");
  }
  sched_fence();
  const u32 lane = threadIdx.x & 63;
  const u32 w = __builtin_amdgcn_readfirstlane(threadIdx.x >> 6) ^ 4;
  const u32 xcd = blockIdx.x & 7, idx = blockIdx.x >> 3;
  const u32 kh = xcd & 1, rg = xcd >> 1, p = idx & 3, panel = rg * 4 + p, m = idx >> 2, j = m - 1;
  const u32 rowO = panel * 131072 + kh * 65536;
  const u32 rot = ROT;
  const u32 a = w & 3;
  u32 xv[4];
#pragma unroll
  for (int i = 0; i < 4; ++i) xv[i] = (lane >> 3) * 1024 + (((lane & 7) ^ (lane >> 4) ^ (4 * (i & 1))) * 16) + i * (8192 - 1024);
  u32 xs = rowO ^ ((a ^ 2) * 32768), wl = a * 4096;
  u32 xs0 = xs + rot * 128, ldr = m != 0 ? w >> 2 : 0;
  asm volatile("" : "+v"(xv[0]), "+v"(xv[1]), "+v"(xv[2]), "+v"(xv[3]));
  asm volatile("" : "+s"(xs), "+s"(wl), "+s"(xs0), "+s"(ldr));
  sched_fence();
  asm volatile("" : "+s"(x), "+s"(w1), "+s"(b1p), "+s"(w2), "+s"(b2p), "+s"(out), "+s"(ws), "+s"(ws2));
  const v4u rx = mk_rsrc(x, 2048 * D * 2);
  const v4u rw1 = mk_rsrc(w1, D * F * 2);
  const u32 w1s = kh * 1792 + j * 256;
  u32 wv[4];
  if (ldr) {
    dma16x4(rx, xv, xs0, wl);
    FSTAMP(1);
    sched_fence();
#pragma unroll
    for (int i = 0; i < 4; ++i) {
      wv[i] = (16 * a + (lane >> 4)) * (F * 2) + (((lane & 15) ^ ((lane >> 4) << 2)) * 16) + i * (4 * F * 2 - 1024);
      pin(wv[i]);
    }
    dma16x4(rw1, wv, w1s + rot * (64 * F * 2), wl + 16384);
    FSTAMP(2);
  }
  sched_fence();
  const v4u rws = mk_rsrc(ws, WS_BYTES);
  const v4u rw2 = mk_rsrc(w2, F * D * 2);
  const u32 pk = panel * 2 + kh, lv16 = lane * 16;
  const u32 epo = WS_CTL + panel * 16384 + 7 * 256, flg = WS_CTL + pk * 8192, sts = WS_STAT + panel * 2048, hpan = WS_H + pk * H_STRIDE;
  const u32 pdst = WS_P + (pk * 8 + m) * P_STRIDE + a * 2048;
  const u32 w2s = kh * 896 * 1024 + m * 128;
  const u32 g = w & 1, c = (w >> 1) & 1;
  const u32 hh = (w >> 2) ^ 1;
  const u32 t31 = lane & 31, h = lane >> 5, q4 = (lane >> 2) & 3;
  const u32 r7 = R7, sw = SW;
  const int rp = RP;
  u32 tag, one;
  auto clear = [&]() {
    const v4u ones = {~0u, ~0u, ~0u, ~0u};
    st16_sc1<0>(ones, rws, lv16, pdst);
    st16_sc1<1024>(ones, rws, lv16, pdst);
  };
  auto mk_w2v = [&](u32 (&v)[2]) {
#pragma unroll
    for (int i = 0; i < 2; ++i) { v[i] = (16 * a + (lane >> 3)) * 1024 + (((lane & 7) ^ ((lane >> 2) & 4)) * 16) + i * (8192 - 1024); pin(v[i]); }
  };
  u32 pollv, sig, sigp, sig2, sigx, cnta, w2c, xr0, xr1, xq[4], ab[7][2], hs[7], fr;
  const u32 polls = 0, r8 = lane >> 3;
  u32 a0, hb;
  int jb4 = r7 * 4096, pos8 = ((int)r7 - rp < 0 ? (int)r7 - rp + 7 : (int)r7 - rp) * 8192;
  auto setup = [&](auto PART) {
    if constexpr (PART.v == 0) {
      int proof = m ? WS_CTL + (pk ^ 1) * 8192 + j * 256 : sts + (64 * (kh ^ 1) + 8 * a) * 16 + 4;
      pin_s(proof);
      proof = hh ? proof : flg;
      pin_s(proof);
      pollv = lane < 7 ? flg + hh * 2048 + lane * 256 : lane == 7 ? proof : OOR;
      pin(pollv);
    }
    if constexpr (PART.v == 1) {
      const u32 p4 = lane & 3, blk = (lane >> 4) & 1;
      a0 = (c ^ sw) * 57344 + (4 * h + q4) * 128 + ((q4 >> 1) * 64) + (4 * (p4 & 1) + 2 * blk + (p4 >> 1)) * 8;
      pin(a0);
    }
    if constexpr (PART.v == 2) {
      sig = L_FLG + 96 + w * 4; sigp = L_FLG + 96 + (w ^ 4) * 4; sig2 = sigp ^ (h * 8);
      cnta = L_FLG + 64 + hh * 4; w2c = L_FLG + 80;
      hb = hpan + (hh * 2 + g) * (56 * 1024) + c * (28 * 1024) + lv16;
      one = 1;
      pin(one); pin(sig); pin(sigp); pin(sig2); pin(cnta); pin(w2c); pin(hb);
    }
    if constexpr (PART.v == 3) {
      const u32 xw = L_RED + a * 8192 + t31 * 256 + (((4 * h) ^ (t31 & 7)) * 16);
#pragma unroll
      for (int q = 0; q < 4; ++q) { xq[q] = xw ^ (q * 16); pin(xq[q]); }
    }
    if constexpr (PART.v >= 4) {
      constexpr int b = PART.v - 4;
      pin_s(jb4); pin_s(pos8);
      ab[b][0] = a0 + pos8; ab[b][1] = ab[b][0] ^ 64;
      hs[b] = hb + jb4;
      pin(ab[b][0]); pin(ab[b][1]); pin(hs[b]);
      jb4 = jb4 == 6 * 4096 ? 0 : jb4 + 4096;
      pos8 = pos8 == 6 * 8192 ? 0 : pos8 + 8192;
    }
  };
  auto setup_all = [&]() {
    setup(IC<0>{}); setup(IC<1>{}); setup(IC<2>{}); setup(IC<3>{}); setup(IC<4>{}); setup(IC<5>{});
    setup(IC<6>{}); setup(IC<7>{}); setup(IC<8>{}); setup(IC<9>{}); setup(IC<10>{});
    xr0 = L_RED + g * 8192 + (16 * c + r8) * 256 + (((2 * (lane & 7)) ^ r8) * 16); xr1 = xr0 ^ 16; sigx = (sig ^ 8) + 64;
    pin(xr0); pin(xr1); pin(sigx);
  };
  auto w2_done = [&]() {
    u32 old = 0;
    wait_vm<0>();
    lds_inc_lane0(old, w2c, one);
    lgkm0_pin(old);
    STAMP(6);
    __builtin_amdgcn_s_setprio(2);
  };

  if (ldr) {
    u32 w2v[2];
    if (a < 2) dma4_sc1(rws, lane == 0 ? 0u : OOR, epo, L_FLG);
    else if (a == 2) dma4(mk_rsrc(b1p, F * 2), lane * 4, w1s, L_B1);
    else dma4(mk_rsrc(b2p, D * 2), lane * 4, m * 128, L_B2);
    mk_w2v(w2v);
    setup_all();
    sched_fence();
    auto dma_g = [&](int kg) {
      const u32 kr = (kg + rot) & 7, sl = slot_of(kg) + wl;
      dma16x4(rx, xv, xs + kr * 128, sl);
      dma16x4(rw1, wv, w1s + kr * (64 * F * 2), sl + 16384);
    };
    auto dma_w2 = [&](int q) {
      int jj = q % 7 + rp;
      pin_s(jj);
      jj = jj >= 7 ? jj - 7 : jj;
      dma16x2(rw2, w2v, w2s + (q >= 7 ? (sw ^ 1) * 65536 : sw * 65536) + jj * 131072, q * 8192 + a * 2048);
    };
    constexpr int ord[14] = {8, 9, 10, 11, 12, 13, 4, 5, 6, 7, 0, 1, 2, 3};
    constexpr int NE = 6 - EARLY0;
    dma_g(1);
    if (CLR_AT == 1) clear();
    FSTAMP(3);
    auto lit = [&](auto KG) {
      constexpr int kg = KG.v;
      wait_vm<8 + (kg > EARLY0 ? 2 : 0) + (kg == 0 ? 1 : 0) + (kg == CLR_AT - 1 ? 2 : 0)>();
      if (kg == 0) FSTAMP(4);
      bar();
      if (kg == 5) dma16_sc1(rws, lv16, sts + (a & 1) * 1024, L_ST + (a & 1) * 1024);
      if (kg < 6) dma_g(kg + 2);
      if (kg >= EARLY0 && kg < 6) dma_w2(ord[kg - EARLY0]);
      if (kg == CLR_AT - 2) clear();
      if (kg == 2) tag = __builtin_amdgcn_readfirstlane(*(const LAS u32*)L_FLG) + 0x01010101u;
    };
    lit(IC<0>{}); lit(IC<1>{}); lit(IC<2>{}); lit(IC<3>{}); lit(IC<4>{}); lit(IC<5>{}); lit(IC<6>{});
    static_assert(NE + N6 <= 10, "");
#pragma unroll
    for (int i = NE; i < NE + N6; ++i) dma_w2(ord[i]);
    wait_vm<2 * N6 + (NE > 0 ? 2 : 0)>();
    bar();
#pragma unroll
    for (int i = NE + N6; i < 14; ++i) dma_w2(ord[i]);
    if (m == 1 && w == 4) st4_sc1(tag, rws, lane == 0 ? 0u : OOR, flg + 16 * 256);
    w2_done();
  } else if (m == 0) {
    v4u xr[2][4];
    u32 ep, w2v[2];
#pragma unroll
    for (int k = 0; k < 8; ++k) ld16(xr[k >> 2][k & 3], rx, xv[0], rowO + w * 8192 + ((k + HROT) & 7) * 128);
    ld4_sc1(ep, rws, 0, epo);
    if (w < 4) clear();
    else if (w == 4) dma4_sc1(rws, lane == 0 ? 0u : OOR, epo, L_FLG);
    else if (w == 5) dma4(mk_rsrc(b2p, D * 2), lane * 4, 0, L_B2);
    else if (w == 6) dma4(rws, lane < 2 ? lane * H_STRIDE : OOR, hpan - 128, L_JUNK);
    else dma4(mk_rsrc(out, 2048 * D * 2), lane == 0 ? 0u : OOR, rowO, L_JUNK);
    float sum = 0.f, sq = 0.f;
    const bf16x2 ones = {(__bf16)1.0f, (__bf16)1.0f};
    wait_vm<0>(ep);
    wait_vm4<0>(xr[0]);
    wait_vm4<0>(xr[1]);
    tag = __builtin_amdgcn_readfirstlane(ep) + 0x01010101u;
#pragma unroll
    for (int k = 0; k < 8; ++k)
#pragma unroll
      for (int e = 0; e < 4; ++e) {
        const bf16x2 v = asb2(xr[k >> 2][k & 3][e]);
        sum = __builtin_amdgcn_fdot2_f32_bf16(v, ones, sum, false);
        sq = __builtin_amdgcn_fdot2_f32_bf16(v, v, sq, false);
      }
#pragma unroll
    for (int o = 1; o < 8; o <<= 1) { sum += __shfl_xor(sum, o); sq += __shfl_xor(sq, o); }
    const float mean = sum * (1.0f / D);
    const float sd = __builtin_amdgcn_sqrtf(fmaxf(sq * (1.0f / D) - mean * mean, 0.f) + 1e-5f);
    st16_sc1<0>(v4u{asu(-mean), tag, asu(sd), tag}, rws, (lane & 7) ? OOR : (64 * kh + 8 * w + (lane >> 3)) * 16, sts);
    STAMP(1);
    bar();
    if (w >= 4) {
      const u32 pv[4] = {lv16, lv16, lv16, lv16};
#pragma nounroll
      for (u32 i = 0; i < HELP_W2; ++i) {
        u32 rb = rg * 14 + i * 4 + a;
        rb = rb >= 56 ? rb - 56 : rb;
        dma16x4(rw2, pv, kh * 896 * 1024 + (p * 56 + rb) * 4096, L_RED + a * 4096);
      }
      mk_w2v(w2v);
#pragma nounroll
      for (int b = 0; b < 7; ++b) {
        const int jj = b + rp >= 7 ? b + rp - 7 : b + rp;
        dma16x2(rw2, w2v, w2s + sw * 65536 + jj * 131072, b * 8192 + a * 2048);
        dma16x2(rw2, w2v, w2s + (sw ^ 1) * 65536 + jj * 131072, (7 + b) * 8192 + a * 2048);
      }
    }
    setup_all();
    if (w >= 4) w2_done();
  } else {
    {
      if (CLR_AT == 0) clear();
      FSTAMP(1);
      u32 xa[2][4], wa[2];
#pragma unroll
      for (int k = 0; k < 4; ++k) {
        xa[0][k] = t31 * 128 + (((2 * k + h) ^ ((t31 >> 1) & 7)) * 16);
        xa[1][k] = xa[0][k] + L_RED;
        pin(xa[0][k]); pin(xa[1][k]);
      }
      wa[0] = 16384 + (8 * h + q4) * 256 + ((w ^ q4) * 64) + ((lane >> 4) & 1) * 32 + (lane & 3) * 8;
      wa[1] = wa[0] + L_RED;
      pin(wa[0]); pin(wa[1]);
      const u32 selw = ((lane & 3) == 0 && ((lane >> 2) & 1) == ((lane >> 4) & 1)) ? 0x3f803f80u : 0u;
      v4u selv = {selw, selw, selw, selw};
      pin(selv);
      const bf16x8 sel = __builtin_bit_cast(bf16x8, selv);
      u32 sta = L_ST + t31 * 16 + (1 - kh) * 1024, stb = sta ^ 1024, b1a = L_B1 + w * 64 + t31 * 2;
      pin(sta); pin(stb); pin(b1a);
      bf16x8 xf[8][4], wf[8];
      f32x16 acc[4];
      f32x4 cs;
      v4u st[4];
      v2u bfr[4];
      f32x2 kk[4], sd[4];
      u32 epl, b1w, bad, hi2, lo2;
      float rs;
      auto rd = [&](auto N, auto PART) {
        constexpr int kg = N.v >> 2, k = N.v & 3, kq = N.v & 7;
        constexpr u32 sl = kg % 3 == 2 ? L_SLOT : 0;
        constexpr int bs = kg % 3 == 1;
        if (PART.v == 0) wf[kq] = lds_tr(wa[bs] + sl + k * 4096, wa[bs] + sl + k * 4096 + 1024);
        else {
          xf[kq][2 * PART.v - 2] = lds_b128(xa[bs][k] + sl + (2 * PART.v - 2) * 4096);
          xf[kq][2 * PART.v - 1] = lds_b128(xa[bs][k] + sl + (2 * PART.v - 1) * 4096);
        }
      };
      auto prep = [&](auto T, auto PART) {
        const u32 nm = st[T.v].x, sdv = st[T.v].z;
        if (PART.v == 0) split2(asf(h ? sdv : nm), hi2, lo2);
        if (PART.v == 1) {
          const u32 y = (hi2 & 0xffffu) | (lo2 & 0xffff0000u);
          bfr[T.v] = v2u{y, y};
          sd[T.v] = f32x2{asf(sdv), asf(sdv)};
          rs = __builtin_amdgcn_rcpf(asf(sdv));
          pin(bfr[T.v]); pin(sd[T.v]); pin(rs);
        }
        if (PART.v == 2) { tile_consts(rs, kk[T.v]); pin(kk[T.v]); }
      };
      auto fill = [&](auto N, auto S) {
        constexpr int n = N.v, s = S.v;
        if constexpr ((n & 3) == 3 && n >= 7 && n < 23 && (s & 1) == 0 && 2 * ((n >> 2) - 1) + s / 2 < 7) setup(IC<4 + 2 * ((n >> 2) - 1) + s / 2>{});
        if constexpr (n == 12 && s == 0) { epl = *(const LAS u32*)L_FLG; b1w = *(const LAS unsigned short*)b1a; }
        if constexpr (n == 23 && s == 0) { tag = __builtin_amdgcn_readfirstlane(epl) + 0x01010101u; b1w <<= 16; pin(b1w); }
        if constexpr (n == 24) st[s] = *(const LAS v4u*)((s < 2 ? sta : stb) + (s & 1) * 512);
        if constexpr (n == 27 && s < 2) {
          const u32 b = (st[2 * s].y ^ tag) | (st[2 * s].w ^ tag) | (st[2 * s + 1].y ^ tag) | (st[2 * s + 1].w ^ tag);
          bad = s ? bad | b : b;
          pin(bad);
        }
        if constexpr (n == 27 && s == 2) {
          if (__builtin_expect(__builtin_amdgcn_ballot_w64(bad != 0) != 0, 0)) {
#pragma nounroll
            for (u32 spin = 0; spin < SPIN_CAP; ++spin) {
#pragma unroll
              for (int t = 0; t < 4; ++t) ld16_sc1_w(st[t], rws, t31 * 16, sts + (((t >> 1) ^ kh ^ 1) * 2 + (t & 1)) * 512);
              bad = 0;
#pragma unroll
              for (int t = 0; t < 4; ++t) bad |= (st[t].y ^ tag) | (st[t].w ^ tag);
              if (__builtin_amdgcn_ballot_w64(bad != 0) == 0) break;
            }
          }
        }
        if constexpr ((n == 28 || n == 29) && s < 3) prep(IC<n - 28>{}, S);
      };
      auto gap = [&](auto N, auto S) {
        constexpr int kg = N.v >> 2, u = (N.v & 3) * 4 + S.v;
        sched_fence();
        if constexpr (kg < 7) { if constexpr (u < 12) rd(IC<4 * kg + u / 3>{}, IC<u % 3>{}); }
        else if constexpr (kg == 7) { if constexpr (u < 8) rd(IC<28 + u / 2>{}, IC<u % 2>{}); }
        fill(N, S);
        sched_fence();
      };
      auto step = [&](auto N) {
        constexpr int n = N.v, mmn = n - 2, k = mmn & 3, kq = mmn & 7;
        if constexpr (mmn >= 0) mfma32<mmn == 0>(acc[0], wf[kq], xf[kq][0]);
        gap(N, IC<0>{});
        if constexpr (mmn >= 0) mfma32<mmn == 0>(acc[1], wf[kq], xf[kq][1]);
        gap(N, IC<1>{});
        if constexpr (mmn >= 0 && mmn < 28 && (mmn == 0 || !AB_NOCS)) mfma16<mmn == 0>(cs, sel, wf[kq]);
        if constexpr (mmn >= 0 && mmn < 28) mfma32<mmn == 0>(acc[2], wf[kq], xf[kq][2]);
        gap(N, IC<2>{});
        if constexpr (mmn >= 0 && mmn < 28) mfma32<mmn == 0>(acc[3], wf[kq], xf[kq][3]);
        if constexpr (mmn == 28 || mmn == 29) { mfma16<false>(cs, sel, wf[2 * mmn - 52]); mfma16<false>(cs, sel, wf[2 * mmn - 51]); }
        gap(N, IC<3>{});
      };
      auto iter = [&](auto KG) {
        constexpr int kg = KG.v;
        if (kg == 2 && CLR_AT == 0) wait_vm<0>();
        wait_lgkm0();
        bar();
        if (kg == 0) STAMP(1);
        FSTAMP(2 + kg);
        step(IC<4 * kg>{}); step(IC<4 * kg + 1>{}); step(IC<4 * kg + 2>{}); step(IC<4 * kg + 3>{});
        if (kg >= KS0 && kg < KS1) __builtin_amdgcn_s_sleep(KSLEEP);
      };
      setup(IC<0>{}); setup(IC<1>{}); setup(IC<2>{}); setup(IC<3>{});
      int hdst = hpan + (w >> 1) * (28 * 1024) + j * 4096 + (w & 1) * 2048, fdst = flg + j * 256;
      u32 cnt = L_FLG + 64, flv = lane < 32 ? lane * 4 : OOR;
      pin_s(hdst); pin_s(fdst); pin(cnt); pin(flv);
      iter(IC<0>{}); iter(IC<1>{}); iter(IC<2>{}); iter(IC<3>{}); iter(IC<4>{}); iter(IC<5>{}); iter(IC<6>{}); iter(IC<7>{});
      FSTAMP(10);
      step(IC<32>{});
      asm volatile("s_nop 5" : "+v"(cs));
      mfma32<false>(acc[0], wf[7], xf[7][0]);
      split2(h ? asf(b1w) : cs[0], hi2, lo2);
      v2u afr = {hi2, lo2};
      pin(afr);
      sched_fence();
      mfma32<false>(acc[1], wf[7], xf[7][1]);
      __builtin_amdgcn_s_setprio(3);
      mfma32k8(acc[0], afr, bfr[0]);
      mfma32k8(acc[1], afr, bfr[1]);
      u32 tagv = tag;
      pin(tagv);
      asm volatile("s_nop 10" : "+v"(acc[0]), "+v"(acc[1]));
      STAMP(2);
      FSTAMP(11);
      auto signal = [&](u32& old, u32 stepi) {
        asm volatile("s_waitcnt lgkmcnt(0)" ::: "memory");
        sched_fence();
        pin(old);
        if (__builtin_amdgcn_readfirstlane(old) == 3) st4_sc1(tagv, rws, flv, fdst + stepi * 2048);
      };
      auto gfrag = [&](auto N) {
        constexpr int t = N.v >> 1, s = N.v & 1;
        v4u o = gelu8<s>(acc[t], kk[t], sd[t]);
        u32 old = 0;
        pin(o);
        if (N.v == PUBF && PUBF > 3) { wait_vm<(PUBF > 3 ? PUBF - 4 : 0)>(); lds_inc_lane0(old, cnt, one); signal(old, 0); }
        st16_sc1<s * 1024>(o, rws, lv16, hdst + t * (56 * 1024));
      };
      gfrag(IC<0>{}); gfrag(IC<1>{}); gfrag(IC<2>{});
      rd(IC<28>{}, IC<2>{}); rd(IC<29>{}, IC<2>{}); rd(IC<30>{}, IC<2>{}); rd(IC<31>{}, IC<2>{});
      sched_fence();
      gfrag(IC<3>{});
      STAMP(3);
      auto late = [&](auto K) {
        mfma32<false>(acc[2], wf[4 + K.v], xf[4 + K.v][2]);
        sched_fence();
        if constexpr (K.v < 3) prep(IC<2 + (2 * K.v) / 3>{}, IC<(2 * K.v) % 3>{});
        sched_fence();
        mfma32<false>(acc[3], wf[4 + K.v], xf[4 + K.v][3]);
        sched_fence();
        if constexpr (K.v < 3) prep(IC<2 + (2 * K.v + 1) / 3>{}, IC<(2 * K.v + 1) % 3>{});
        sched_fence();
      };
      late(IC<0>{}); late(IC<1>{}); late(IC<2>{}); late(IC<3>{});
      mfma32k8(acc[2], afr, bfr[2]);
      mfma32k8(acc[3], afr, bfr[3]);
      asm volatile("s_nop 10" : "+v"(acc[2]), "+v"(acc[3]));
      if (PUBF == 3) { u32 o3 = 0; wait_vm<0>(); lds_inc_lane0(o3, cnt, one); signal(o3, 0); }
      gfrag(IC<4>{}); gfrag(IC<5>{});
      STAMP(4);
      gfrag(IC<6>{}); gfrag(IC<7>{});
      STAMP(5);
      u32 old = 0;
      wait_vm<0>();
      lds_inc_lane0(old, cnt + 4, one);
      signal(old, 1);
      __builtin_amdgcn_s_setprio(0);
      STAMP(6);
    }
  }

  {
    v4u hf[28];
    auto ld_h = [&](auto I) { ld16_sc1<(I.v & 3) * 1024>(hf[I.v], rws, hs[I.v >> 2], 0); };
    bf16x8 wb[28][2];
    auto rd_w = [&](auto I, auto N) {
      const u32 o = ab[I.v >> 2][N.v] + (I.v & 3) * 2048;
      wb[I.v][N.v] = lds_tr(o, o + 1024);
    };
    lds_wait<1, 4>(w2c);
    rd_w(IC<0>{}, IC<1>{}); rd_w(IC<0>{}, IC<0>{}); rd_w(IC<1>{}, IC<1>{}); rd_w(IC<1>{}, IC<0>{});
    u32 fv;
    if (m == 0) {
      if (hh) lds_wait<4, 1>(sigp);
      else {
        fv = ~tag;
        poll_eq<1, 4>(fv, rws, 0, flg + 16 * 256, tag);
        __builtin_amdgcn_s_sleep(DHF);
      }
      ld4_sc1_w(fv, rws, pollv, polls);
      poll_eq<255, POLL_SLEEP>(fv, rws, pollv, polls, tag);
    } else {
      lds_wait<1, 4>(cnta);
    }
    STAMP(7);
    ld_h(IC<0>{}); ld_h(IC<1>{}); ld_h(IC<2>{}); ld_h(IC<3>{});
    if (m != 0) {
      if (hh) lds_wait<1, 1>(sigp); else __builtin_amdgcn_s_sleep(DF);
      ld4_sc1_w(fv, rws, pollv, polls);
      poll_eq<255, POLL_SLEEP>(fv, rws, pollv, polls, tag);
    }
    STAMP(8);
    constexpr int WIN = WINDOW;
    ld_h(IC<4>{}); ld_h(IC<5>{}); ld_h(IC<6>{}); ld_h(IC<7>{});
    if constexpr (WIN > 8) { ld_h(IC<8>{}); ld_h(IC<9>{}); }
    if constexpr (WIN > 10) { ld_h(IC<10>{}); ld_h(IC<11>{}); }
    if constexpr (WIN > 12) { ld_h(IC<12>{}); ld_h(IC<13>{}); }
    static_assert(WIN == 8 || WIN == 10 || WIN == 12 || WIN == 14, "");
    f32x16 acc0, acc1;
    auto gstamp = [&](auto K) { GSTAMP(K.v); };
    auto g2 = [&](auto I) {
      constexpr int i = I.v;
      wait_vm<(i + WIN < 28 ? WIN - 1 : 27 - i)>(hf[i]);
      mfma32<i == 0>(acc1, wb[i][1], __builtin_bit_cast(bf16x8, hf[i]));
      if constexpr ((i & 3) == 0) gstamp(IC<1 + i / 4>{});
      sched_fence();
      if constexpr (i + WIN < 28) ld_h(IC<i + WIN>{});
      if constexpr (i == GOAT) *(volatile LAS u32*)sig = one;
      if constexpr (i == 1) *(volatile LAS u32*)(sig + 32) = one;
      if constexpr (i == 22) fr = *(volatile LAS u32*)(sig2 + 96);
      if constexpr (ARR == 0 || i + WIN >= 28) rd_w(IC<i + 2>{}, IC<1>{});
      sched_fence();
      if (PAD1 >= 0) asm volatile("s_nop %0" ::"i"(PAD1));
      mfma32<i == 0>(acc0, wb[i][0], __builtin_bit_cast(bf16x8, hf[i]));
      sched_fence();
      if constexpr (ARR == 1 && i + WIN < 28) rd_w(IC<i + 2>{}, IC<1>{});
      rd_w(IC<i + 2>{}, IC<0>{});
      sched_fence();
      if (PAD2 >= 0) asm volatile("s_nop %0" ::"i"(PAD2));
    };
    g2(IC<0>{}); g2(IC<1>{}); g2(IC<2>{}); g2(IC<3>{}); g2(IC<4>{}); g2(IC<5>{}); g2(IC<6>{});
    g2(IC<7>{}); g2(IC<8>{}); g2(IC<9>{}); g2(IC<10>{}); g2(IC<11>{}); g2(IC<12>{}); g2(IC<13>{});
    g2(IC<14>{}); g2(IC<15>{}); g2(IC<16>{}); g2(IC<17>{}); g2(IC<18>{}); g2(IC<19>{}); g2(IC<20>{});
    g2(IC<21>{}); g2(IC<22>{}); g2(IC<23>{});
    auto tail1 = [&](auto I) {
      wait_vm<27 - I.v>(hf[I.v]);
      mfma32<false>(acc1, wb[I.v][1], __builtin_bit_cast(bf16x8, hf[I.v]));
      sched_fence();
      if constexpr (I.v < 26) rd_w(IC<I.v + 2>{}, IC<1>{}); else rd_w(I, IC<0>{});
      sched_fence();
    };
    tail1(IC<24>{}); tail1(IC<25>{}); tail1(IC<26>{}); tail1(IC<27>{});
    mfma32<false>(acc0, wb[24][0], __builtin_bit_cast(bf16x8, hf[24]));
    if (hh && __builtin_amdgcn_ballot_w64(fr != 1) != 0) lds_wait<0, 1>(sig2 + 96);
    mfma32<false>(acc0, wb[25][0], __builtin_bit_cast(bf16x8, hf[25]));
    asm volatile("s_nop 7" : "+v"(acc1));
    auto wr = [&](const f32x16& v, int q, u32 off) { *(LAS f32x4*)(xq[q] + off) = f32x4{v[4 * q], v[4 * q + 1], v[4 * q + 2], v[4 * q + 3]}; };
    mfma32<false>(acc0, wb[26][0], __builtin_bit_cast(bf16x8, hf[26]));
    sched_fence();
    wr(acc1, 0, 128); wr(acc1, 1, 128);
    sched_fence();
    mfma32<false>(acc0, wb[27][0], __builtin_bit_cast(bf16x8, hf[27]));
    sched_fence();
    wr(acc1, 2, 128); wr(acc1, 3, 128);
    STAMP(9);
    asm volatile("s_nop 9" : "+v"(acc0)::"memory");
    wr(acc0, 0, 0); wr(acc0, 1, 0); wr(acc0, 2, 0); wr(acc0, 3, 0);
    asm volatile("" ::: "memory");
    *(volatile LAS u32*)(sig + 64) = one;
    STAMP(10);
    if (hh == 0) {
      f32x4 sum[4];
      auto gather = [&]() {
        f32x4 u[4], v[4];
#pragma unroll
        for (int q = 0; q < 4; ++q) {
          u[q] = lds_f4(((q & 1) ? xr1 : xr0) + (q >> 1) * 2048);
          v[q] = lds_f4(((q & 1) ? xr1 : xr0) + (q >> 1) * 2048 + 16384);
        }
#pragma unroll
        for (int q = 0; q < 4; ++q) sum[q] = add4(u[q], v[q]);
      };
      lds_wait<0, 1>(sigx);
      gather();
      asm volatile("" ::: "memory");
      *(volatile LAS u32*)(sig + 96) = one;
      v4u d[2];
#pragma unroll
      for (int q = 0; q < 4; ++q) {
        d[q >> 1][2 * (q & 1)] = min(pack2(sum[q][0], sum[q][1]), 0xfffffffeu);
        d[q >> 1][2 * (q & 1) + 1] = min(pack2(sum[q][2], sum[q][3]), 0xfffffffeu);
      }
      pin(d[0]); pin(d[1]);
      st16_sc1<0>(d[0], rws, lv16, pdst); st16_sc1<1024>(d[1], rws, lv16, pdst);
      STAMP(11);
      __builtin_amdgcn_s_setprio(0);
      const u32 psrc = WS_P + ((pk ^ 1) * 8 + m) * P_STRIDE + a * 2048;
      const v4u bb = *(const LAS v4u*)(L_B2 + (lane & 7) * 16);
      lds_wait<1, 1>(sigp + 32);
      wait_vm<0>();
      if (DP) __builtin_amdgcn_s_sleep(DP);
      ld16_sc1<0>(d[0], rws, lv16, psrc); ld16_sc1<1024>(d[1], rws, lv16, psrc);
      STAMP(12);
      const u32 b0 = bb[0], b1 = bb[1], b2 = bb[2], b3 = bb[3];
      f32x4 bl = {bf_lo(b0), bf_hi(b0), bf_lo(b1), bf_hi(b1)}, bh = {bf_lo(b2), bf_hi(b2), bf_lo(b3), bf_hi(b3)};
      u32 ov = (32 * g + 16 * c + r8) * 1024 + (lane & 7) * 16;
      pin(bl); pin(bh); pin(ov);
      wait_vm<0>(d[0]);
      pin(d[1]);
      auto unsent = [&]() {
        const u32 mx = max(max(max(d[0][0], d[0][1]), max(d[0][2], d[0][3])), max(max(d[1][0], d[1][1]), max(d[1][2], d[1][3])));
        return __builtin_amdgcn_ballot_w64(mx == ~0u) != 0;
      };
      if (__builtin_expect(unsent(), 0)) {
#pragma nounroll
        for (u32 spin = 0; spin < SPIN_CAP; ++spin) {
          __builtin_amdgcn_s_sleep(POLL_SLEEP);
          ld32_sc1_w(d, rws, lv16, psrc);
          if (!unsent()) break;
        }
      }
      STAMP(5);
      f32x4 pb[4];
#pragma unroll
      for (int q = 0; q < 4; ++q) {
        const u32 p0 = d[q >> 1][2 * (q & 1)], p1 = d[q >> 1][2 * (q & 1) + 1];
        pb[q] = add4(f32x4{bf_lo(p0), bf_hi(p0), bf_lo(p1), bf_hi(p1)}, (q & 1) ? bh : bl);
      }
      lds_wait<WSL, 1>(sig2 + 64);
      v4u o[2];
      {
        f32x4 u[4], v[4];
#pragma unroll
        for (int q = 0; q < 4; ++q) {
          u[q] = lds_f4(((q & 1) ? xr1 : xr0) + (q >> 1) * 2048);
          v[q] = lds_f4(((q & 1) ? xr1 : xr0) + (q >> 1) * 2048 + 16384);
        }
#pragma unroll
        for (int q = 0; q < 4; ++q) {
          const f32x4 r = (pb[q] + u[q]) + v[q];
          o[q >> 1][2 * (q & 1)] = pack2(r[0], r[1]);
          o[q >> 1][2 * (q & 1) + 1] = pack2(r[2], r[3]);
        }
      }
      STAMP(13);
      const v4u ro = mk_rsrc(out, 2048 * D * 2);
      st16_out(o[0], ro, ov, rowO + m * 128);
      st16_out(o[1], ro, ov, rowO + m * 128 + 8 * 1024);
      if (kh == 0 && m == 0 && w == 4) st4_sc1(tag, rws, lane == 0 ? 0u : OOR, epo);
    }
  }
}

extern "C" void kernel_launch(void* const* d_in, const int* in_sizes, int n_in, void* d_out, int out_size, void* d_ws,
                              size_t ws_size, hipStream_t stream) {
  (void)in_sizes; (void)n_in; (void)out_size; (void)ws_size;
  (void)hipFuncSetAttribute((const void*)mixer_mlp, hipFuncAttributeMaxDynamicSharedMemorySize, L_BYTES);
  mixer_mlp<<<256, 512, L_BYTES, stream>>>((const char*)d_in[0], (const char*)d_in[1], (const char*)d_in[2], (const char*)d_in[3],
                                           (const char*)d_in[4], (char*)d_out, (char*)d_ws, (char*)d_ws);
}
```

```cpp
#include <hip/hip_runtime.h>
#include <stdint.h>

typedef unsigned u32;
typedef unsigned v2u __attribute__((ext_vector_type(2)));
typedef unsigned v4u __attribute__((ext_vector_type(4)));
typedef short s16x4 __attribute__((ext_vector_type(4)));
typedef __bf16 bf16x2 __attribute__((ext_vector_type(2)));
typedef __bf16 bf16x8 __attribute__((ext_vector_type(8)));
typedef float f32x2 __attribute__((ext_vector_type(2)));
typedef float f32x4 __attribute__((ext_vector_type(4)));
typedef float f32x16 __attribute__((ext_vector_type(16)));
#define LAS __attribute__((address_space(3)))
#define DI __device__ __forceinline__

namespace {
constexpr int D = 512, F = 1792;
constexpr u32 WS_CTL = 0;
constexpr u32 WS_STAT = 1u << 18;
constexpr u32 WS_H = 1u << 20;
constexpr u32 H_STRIDE = 4 * 56 * 1024 + 256;
constexpr u32 WS_P = 9u << 20;
constexpr u32 P_STRIDE = 8192 + 256;
constexpr u32 WS_BYTES = 12u << 20;
constexpr u32 L_SLOT = 24576;
constexpr u32 L_XO = 90112;
constexpr u32 L_RED = 114688;
constexpr u32 L_ST = 155648;
constexpr u32 L_B1 = L_ST + 2048;
constexpr u32 L_B2 = L_B1 + 256;
constexpr u32 L_FLG = L_B2 + 256;
constexpr u32 L_JUNK = L_FLG + 256;
constexpr u32 L_BYTES = 163840;
constexpr u32 CODE_LINES = 137;
constexpr u32 SPIN_CAP = 1u << 14;
constexpr u32 OOR = 0x7ffffff0u;

DI v4u mk_rsrc(const void* p, u32 bytes) { return v4u{(u32)(uint64_t)p, (u32)((uint64_t)p >> 32) & 0xffffu, bytes, 0x00020000u}; }
DI void dma16x4(v4u rs, const u32 (&v)[4], u32 soff, u32 lds) {
  asm volatile("s_mov_b32 m0, %6\n\ts_nop 0\n\tbuffer_load_dwordx4 %0, %4, %5 offen lds\n\tbuffer_load_dwordx4 %1, %4, %5 offen offset:1024 lds\n\t"
               "buffer_load_dwordx4 %2, %4, %5 offen offset:2048 lds\n\tbuffer_load_dwordx4 %3, %4, %5 offen offset:3072 lds"
               ::"v"(v[0]), "v"(v[1]), "v"(v[2]), "v"(v[3]), "s"(rs), "s"(soff), "s"(lds) : "memory");
}
DI void dma16x2(v4u rs, const u32 (&v)[2], u32 soff, u32 lds) {
  asm volatile("s_mov_b32 m0, %4\n\ts_nop 0\n\tbuffer_load_dwordx4 %0, %2, %3 offen lds\n\tbuffer_load_dwordx4 %1, %2, %3 offen offset:1024 lds"
               ::"v"(v[0]), "v"(v[1]), "s"(rs), "s"(soff), "s"(lds) : "memory");
}
DI void dma4(v4u rs, u32 voff, u32 soff, u32 lds) {
  asm volatile("s_mov_b32 m0, %3\n\ts_nop 0\n\tbuffer_load_dword %0, %1, %2 offen lds" ::"v"(voff), "s"(rs), "s"(soff), "s"(lds) : "memory");
}
DI void dma4_sc1(v4u rs, u32 voff, u32 soff, u32 lds) {
  asm volatile("s_mov_b32 m0, %3\n\ts_nop 0\n\tbuffer_load_dword %0, %1, %2 offen sc1 lds" ::"v"(voff), "s"(rs), "s"(soff), "s"(lds) : "memory");
}
DI void dma16_sc1(v4u rs, u32 voff, u32 soff, u32 lds) {
  asm volatile("s_mov_b32 m0, %3\n\ts_nop 0\n\tbuffer_load_dwordx4 %0, %1, %2 offen sc1 lds" ::"v"(voff), "s"(rs), "s"(soff), "s"(lds) : "memory");
}
template <int OFF> DI void ld16_sc1(v4u& d, v4u rs, u32 voff, u32 soff) {
  asm volatile("buffer_load_dwordx4 %0, %1, %2, %3 offen offset:%4 sc1" : "=v"(d) : "v"(voff), "s"(rs), "s"(soff), "i"(OFF) : "memory");
}
DI void ld16(v4u& d, v4u rs, u32 voff, u32 soff) {
  asm volatile("buffer_load_dwordx4 %0, %1, %2, %3 offen" : "=v"(d) : "v"(voff), "s"(rs), "s"(soff) : "memory");
}
DI void ld4_sc1(u32& d, v4u rs, u32 voff, u32 soff) {
  asm volatile("buffer_load_dword %0, %1, %2, %3 offen sc1" : "=v"(d) : "v"(voff), "s"(rs), "s"(soff) : "memory");
}
DI void ld16_sc1_w(v4u& d, v4u rs, u32 voff, u32 soff) {
  asm volatile("buffer_load_dwordx4 %0, %1, %2, %3 offen sc1\n\ts_waitcnt vmcnt(0)" : "=&v"(d) : "v"(voff), "s"(rs), "s"(soff) : "memory");
}
DI void ld4_sc1_w(u32& d, v4u rs, u32 voff, u32 soff) {
  asm volatile("buffer_load_dword %0, %1, %2, %3 offen sc1\n\ts_waitcnt vmcnt(0)" : "=&v"(d) : "v"(voff), "s"(rs), "s"(soff) : "memory");
}
DI void ld32_sc1_w(v4u (&d)[2], v4u rs, u32 voff, u32 soff) {
  asm volatile("buffer_load_dwordx4 %0, %2, %3, %4 offen sc1\n\tbuffer_load_dwordx4 %1, %2, %3, %4 offen offset:1024 sc1\n\ts_waitcnt vmcnt(0)"
               : "=&v"(d[0]), "=&v"(d[1]) : "v"(voff), "s"(rs), "s"(soff) : "memory");
}
template <int OFF> DI void st16_sc1(v4u d, v4u rs, u32 voff, u32 soff) {
  asm volatile("buffer_store_dwordx4 %0, %1, %2, %3 offen offset:%4 sc1\n\ts_nop 1" ::"v"(d), "v"(voff), "s"(rs), "s"(soff), "i"(OFF) : "memory");
}
#define OUT_FLAGS " sc0 sc1"
#define POLL_SLEEP 3
DI void st16_out(v4u d, v4u rs, u32 voff, u32 soff) {
  asm volatile("buffer_store_dwordx4 %0, %1, %2, %3 offen" OUT_FLAGS "\n\ts_nop 1" ::"v"(d), "v"(voff), "s"(rs), "s"(soff) : "memory");
}
DI void st4_sc1(u32 d, v4u rs, u32 voff, u32 soff) {
  asm volatile("buffer_store_dword %0, %1, %2, %3 offen sc1" ::"v"(d), "v"(voff), "s"(rs), "s"(soff) : "memory");
}
template <int N> DI void wait_vm() { asm volatile("s_waitcnt vmcnt(%0)" ::"i"(N) : "memory"); }
template <int N, class T> DI void wait_vm(T& r) {
  wait_vm<N>();
  __builtin_amdgcn_sched_barrier(0);
  asm volatile("" : "+v"(r));
}
template <int N> DI void wait_vm4(v4u (&r)[4]) {
  wait_vm<N>();
  __builtin_amdgcn_sched_barrier(0);
  asm volatile("" : "+v"(r[0]), "+v"(r[1]), "+v"(r[2]), "+v"(r[3]));
}
DI void lds_inc_lane0(u32& old, u32 addr, u32 one) {
  asm volatile("s_mov_b64 exec, 1\n\tds_add_rtn_u32 %0, %1, %2\n\ts_mov_b64 exec, -1" : "+v"(old) : "v"(addr), "v"(one) : "memory");
}
DI void wait_lgkm0() { __builtin_amdgcn_s_waitcnt(0xC07F); asm volatile("" ::: "memory"); }
DI void bar() { asm volatile("s_barrier" ::: "memory"); }
DI void sched_fence() { __builtin_amdgcn_sched_barrier(0); }
template <class T> DI void pin(T& v) { asm volatile("" : "+v"(v)); }

DI void lds_add_lane0(u32 addr, u32 one) {
  asm volatile("s_mov_b64 exec, 1\n\tds_add_u32 %0, %1\n\ts_mov_b64 exec, -1" ::"v"(addr), "v"(one) : "memory");
}
DI void lgkm0_pin(u32& v) { asm volatile("s_waitcnt lgkmcnt(0)" : "+v"(v)::"memory"); }
DI void pin_s(int& v) { asm volatile("" : "+s"(v)); }
template <int SLEEP, int VAL> DI void lds_wait(u32 addr) {
  u32 t, n;
  asm volatile("s_mov_b32 %1, %5\n1:\n\tds_read_b32 %0, %2\n\ts_waitcnt lgkmcnt(0)\n\tv_cmp_ne_u32 vcc, %3, %0\n\ts_nop 0\n\ts_cmp_eq_u64 vcc, 0\n\t"
               "s_cbranch_scc1 2f\n\ts_sleep %4\n\ts_sub_u32 %1, %1, 1\n\ts_cbranch_scc0 1b\n2:"
               : "=&v"(t), "=&s"(n) : "v"(addr), "i"(VAL), "i"(SLEEP), "i"(SPIN_CAP << 4) : "memory", "scc", "vcc");
}
DI void flag_set(u32 tag, v4u rs, u32 voff, u32 soff) {
  asm volatile("buffer_atomic_swap %0, %1, %2, %3 offen" ::"v"(tag), "v"(voff), "s"(rs), "s"(soff) : "memory");
}
template <int SLEEP> DI void flag_wait(v4u rs, u32 voff, u32 mask, u32 tag) {
  u32 f, n;
  asm volatile("s_mov_b32 %1, %7\n1:\n\tv_mov_b32 %0, 0\n\tbuffer_atomic_or %0, %2, %3, 0 offen sc0\n\ts_waitcnt vmcnt(0)\n\t"
               "v_cmp_ne_u32 vcc, %5, %0\n\ts_nop 0\n\ts_and_b32 vcc_lo, vcc_lo, %4\n\ts_cbranch_scc0 2f\n\ts_sleep %6\n\t"
               "s_sub_u32 %1, %1, 1\n\ts_cbranch_scc0 1b\n2:"
               : "=&v"(f), "=&s"(n) : "v"(voff), "s"(rs), "s"(mask), "s"(tag), "i"(SLEEP), "i"(SPIN_CAP) : "memory", "scc", "vcc");
}
DI bf16x8 lds_b128(u32 a) { return *(const LAS bf16x8*)a; }
DI f32x4 lds_f4(u32 a) { return *(const LAS f32x4*)a; }
DI bf16x8 lds_tr(u32 a0, u32 a1) {
  s16x4 lo = __builtin_amdgcn_ds_read_tr16_b64_v4i16((LAS s16x4*)a0);
  s16x4 hi = __builtin_amdgcn_ds_read_tr16_b64_v4i16((LAS s16x4*)a1);
  return __builtin_bit_cast(bf16x8, __builtin_shufflevector(lo, hi, 0, 1, 2, 3, 4, 5, 6, 7));
}
template <bool FIRST> DI void mfma32(f32x16& c, bf16x8 a, bf16x8 b) {
  if (FIRST) asm volatile("v_mfma_f32_32x32x16_bf16 %0, %1, %2, 0" : "=&v"(c) : "v"(a), "v"(b));
  else asm volatile("v_mfma_f32_32x32x16_bf16 %0, %1, %2, %0" : "+v"(c) : "v"(a), "v"(b));
}
template <bool FIRST> DI void mfma16(f32x4& c, bf16x8 a, bf16x8 b) {
  if (FIRST) asm volatile("v_mfma_f32_16x16x32_bf16 %0, %1, %2, 0" : "=&v"(c) : "v"(a), "v"(b));
  else asm volatile("v_mfma_f32_16x16x32_bf16 %0, %1, %2, %0" : "+v"(c) : "v"(a), "v"(b));
}
DI void nop11(f32x16& a, f32x16& b) { asm volatile("s_nop 10" : "+v"(a), "+v"(b)); }
DI u32 pack2(float a, float b) { bf16x2 v = {(__bf16)a, (__bf16)b}; return __builtin_bit_cast(u32, v); }
DI float asf(u32 w) { return __builtin_bit_cast(float, w); }
DI u32 asu(float f) { return __builtin_bit_cast(u32, f); }
DI bf16x2 asb2(u32 w) { return __builtin_bit_cast(bf16x2, w); }
DI float bf_lo(u32 w) { return asf(w << 16); }
DI float bf_hi(u32 w) { return asf(w & 0xffff0000u); }

template <int S> DI v4u gelu8(const f32x16& acc, f32x2 kk, f32x2 sd) {
  f32x2 p[4];
#define PAIR(i) __builtin_shufflevector(acc, acc, 8 * S + 2 * i, 8 * S + 2 * i + 1)
  asm volatile(
      "v_pk_mul_f32 %0, %5, %5\n\tv_pk_mul_f32 %1, %6, %6\n\tv_pk_mul_f32 %2, %7, %7\n\tv_pk_mul_f32 %3, %8, %8\n\t"
      "v_pk_fma_f32 %0, %0, %4, %4 op_sel:[0,0,1] op_sel_hi:[1,0,1]\n\tv_pk_fma_f32 %1, %1, %4, %4 op_sel:[0,0,1] op_sel_hi:[1,0,1]\n\t"
      "v_pk_fma_f32 %2, %2, %4, %4 op_sel:[0,0,1] op_sel_hi:[1,0,1]\n\tv_pk_fma_f32 %3, %3, %4, %4 op_sel:[0,0,1] op_sel_hi:[1,0,1]\n\t"
      "v_pk_mul_f32 %0, %0, %5\n\tv_pk_mul_f32 %1, %1, %6\n\tv_pk_mul_f32 %2, %2, %7\n\tv_pk_mul_f32 %3, %3, %8"
      : "=&v"(p[0]), "=&v"(p[1]), "=&v"(p[2]), "=&v"(p[3])
      : "v"(kk), "v"(PAIR(0)), "v"(PAIR(1)), "v"(PAIR(2)), "v"(PAIR(3)));
#pragma unroll
  for (int i = 0; i < 4; ++i) p[i] = f32x2{__builtin_amdgcn_exp2f(p[i][0]), __builtin_amdgcn_exp2f(p[i][1])};
  sched_fence();
#pragma unroll
  for (int i = 0; i < 4; ++i) p[i] = __builtin_elementwise_fma(p[i], sd, sd);
  sched_fence();
#pragma unroll
  for (int i = 0; i < 4; ++i) p[i] = f32x2{__builtin_amdgcn_rcpf(p[i][0]), __builtin_amdgcn_rcpf(p[i][1])};
  sched_fence();
  p[0] *= PAIR(0); p[1] *= PAIR(1); p[2] *= PAIR(2); p[3] *= PAIR(3);
#undef PAIR
  sched_fence();
  v4u o;
#pragma unroll
  for (int i = 0; i < 4; ++i) o[i] = pack2(p[i][0], p[i][1]);
  return o;
}
DI void split2(float y, u32& hi2, u32& lo2) {
  float r;
  asm volatile("v_cvt_pk_bf16_f32 %0, %3, %3\n\tv_and_b32 %2, 0xffff0000, %0\n\tv_sub_f32 %2, %3, %2\n\tv_cvt_pk_bf16_f32 %1, %2, %2"
               : "=&v"(hi2), "=&v"(lo2), "=&v"(r) : "v"(y));
}
DI void tile_consts(float rs, f32x2& kk) {
  float k1, k2;
  asm volatile("v_mul_f32 %1, %2, %2\n\tv_mul_f32 %0, 0xc0135761, %2\n\tv_mul_f32 %1, %1, %2\n\tv_mul_f32 %1, 0xbdd2d3e8, %1"
               : "=&v"(k1), "=&v"(k2) : "v"(rs));
  kk = f32x2{k2, k1};
}
DI f32x4 add4(f32x4 a, f32x4 o) {
  float r[4];
  asm volatile("v_add_f32 %0, %4, %8\n\tv_add_f32 %1, %5, %9\n\tv_add_f32 %2, %6, %10\n\tv_add_f32 %3, %7, %11"
               : "=&v"(r[0]), "=&v"(r[1]), "=&v"(r[2]), "=v"(r[3])
               : "v"(a[0]), "v"(a[1]), "v"(a[2]), "v"(a[3]), "v"(o[0]), "v"(o[1]), "v"(o[2]), "v"(o[3]));
  return f32x4{r[0], r[1], r[2], r[3]};
}
DI void mfma32k8(f32x16& c, v2u a, v2u b) { asm volatile("v_mfma_f32_32x32x8_bf16 %0, %1, %2, %0" : "+v"(c) : "v"(a), "v"(b)); }

#define STAMP(i)
#define FSTAMP(i)
#define GSTAMP(i)
template <int V> struct IC { static constexpr int v = V; };
#define ROT ((rg + 4 * ((p + j) & 1)) & 7)
#define HROT (rg + 2)
#define R7 (m ? j : 0)
#define SW (p >> 1)
#define RP (rg + 3 * (p & 1))
#define HELP_W2 6
#define CLR_AT 3
#define N7 4
#define N8 4
#define S9 12
#define GOAT 8
#define WINDOW 12
#define DF 14
#define DO 8
#define DHF 0
#define DHO 0
#define DP 0
#define S2PAD -1
#define WSL 1
#define ARR 0
#define PAD1 -1
#define PAD2 -1
}

__global__ __launch_bounds__(512, 2) void mixer_mlp(const char* x, const char* w1, const char* b1p, const char* w2, const char* b2p,
                                                    char* out, char* ws, char* ws2) {
  {
    uint64_t pc;
    u32 o, so;
    asm volatile("s_getpc_b64 %0\n\ts_lshl_b32 %2, %4, 4\n\tv_lshl_add_u32 %1, %3, 12, %2\n\tv_min_u32 %1, %5, %1\n\ts_mov_b32 m0, %6\n\ts_nop 0\n\t"
                 "global_load_lds_dword %1, %0"
                 : "=&s"(pc), "=&v"(o), "=&s"(so) : "v"(threadIdx.x), "s"(blockIdx.x), "i"(CODE_LINES * 128 - 128), "i"(L_JUNK) : "memory", "scc");
  }
  sched_fence();
  const u32 lane = threadIdx.x & 63;
  const u32 w = __builtin_amdgcn_readfirstlane(threadIdx.x >> 6) ^ 4;
  const u32 xcd = blockIdx.x & 7, idx = blockIdx.x >> 3;
  const u32 kh = xcd & 1, rg = xcd >> 1, p = idx & 3, panel = rg * 4 + p, m = idx >> 2, j = m - 1;
  const u32 rowO = panel * 131072 + kh * 65536;
  const u32 rot = ROT;
  const u32 a = w & 3;
  u32 xv[2];
#pragma unroll
  for (int i = 0; i < 2; ++i) xv[i] = (lane >> 3) * 1024 + (((lane & 7) ^ (lane >> 4) ^ (4 * i)) * 16) + i * (8192 - 1024);
  u32 xs = (rowO ^ 65536) + a * 16384, wl = a * 2048;
  u32 xs0 = xs + rot * 128, ldr = m != 0 ? w >> 2 : 0;
  asm volatile("" : "+v"(xv[0]), "+v"(xv[1]));
  asm volatile("" : "+s"(xs), "+s"(wl), "+s"(xs0), "+s"(ldr));
  sched_fence();
  asm volatile("" : "+s"(x), "+s"(w1), "+s"(b1p), "+s"(w2), "+s"(b2p), "+s"(out), "+s"(ws), "+s"(ws2));
  const v4u rx = mk_rsrc(x, 2048 * D * 2);
  const v4u rw1 = mk_rsrc(w1, D * F * 2);
  const u32 w1s = kh * 1792 + j * 256;
  u32 wv[4];
  if (ldr) {
    dma16x2(rx, xv, xs0, wl);
    FSTAMP(1);
    sched_fence();
#pragma unroll
    for (int i = 0; i < 4; ++i) {
      wv[i] = (16 * a + (lane >> 4)) * (F * 2) + (((lane & 15) ^ ((lane >> 4) << 2)) * 16) + i * (4 * F * 2 - 1024);
      pin(wv[i]);
    }
    dma16x4(rw1, wv, w1s + rot * (64 * F * 2), 2 * wl + 8192);
    FSTAMP(2);
  }
  sched_fence();
  const v4u rws = mk_rsrc(ws, WS_BYTES);
  const v4u rw2 = mk_rsrc(w2, F * D * 2);
  const u32 pk = panel * 2 + kh, lv16 = lane * 16;
  const u32 epo = WS_CTL + panel * 2048 + 512, flg = WS_CTL + pk * 1024, sts = WS_STAT + panel * 2048, hpan = WS_H + pk * H_STRIDE;
  const u32 pdst = WS_P + (pk * 8 + m) * P_STRIDE + a * 2048;
  const u32 w2s = kh * 896 * 1024 + m * 128;
  const u32 g = w & 1, c = (w >> 1) & 1;
  const u32 hh = (w >> 2) ^ 1;
  const u32 t31 = lane & 31, h = lane >> 5, q4 = (lane >> 2) & 3;
  const u32 r7 = R7, sw = SW;
  const int rp = RP;
  u32 tag, one;
  auto clear = [&]() {
    const v4u ones = {~0u, ~0u, ~0u, ~0u};
    st16_sc1<0>(ones, rws, lv16, pdst);
    st16_sc1<1024>(ones, rws, lv16, pdst);
  };
  auto mk_w2v = [&](u32 (&v)[2]) {
#pragma unroll
    for (int i = 0; i < 2; ++i) { v[i] = (16 * a + (lane >> 3)) * 1024 + (((lane & 7) ^ ((lane >> 2) & 4)) * 16) + i * (8192 - 1024); pin(v[i]); }
  };
  u32 pollv, sig, sigp, sig2, sigx, cnta, w2c, xr0, xr1, xq[4], ab[7][2], hs[7], fr;
  const u32 r8 = lane >> 3;
  u32 a0, hb;
  int jb4 = r7 * 4096, pos8 = ((int)r7 - rp < 0 ? (int)r7 - rp + 7 : (int)r7 - rp) * 8192;
  auto setup = [&](auto PART) {
    if constexpr (PART.v == 0) {
      int proof = (flg ^ 1024) + (m ? j * 4 : 32) - 28, nl = hh ? (m ? 8 : 11) : 7;
      pin_s(proof); pin_s(nl);
      pollv = lane < 7 ? flg + hh * 256 : proof;
      pollv = (int)lane < nl ? pollv + lane * 4 : OOR;
      pin(pollv);
    }
    if constexpr (PART.v == 1) {
      const u32 p4 = lane & 3, blk = (lane >> 4) & 1;
      a0 = (c ^ sw) * 57344 + (4 * h + q4) * 128 + ((q4 >> 1) * 64) + (4 * (p4 & 1) + 2 * blk + (p4 >> 1)) * 8;
      pin(a0);
    }
    if constexpr (PART.v == 2) {
      sig = L_FLG + 96 + w * 4; sigp = L_FLG + 96 + (w ^ 4) * 4; sig2 = sigp ^ (h * 8);
      cnta = L_FLG + 64; w2c = L_FLG + 80;
      hb = hpan + (hh * 2 + g) * (56 * 1024) + c * (28 * 1024) + lv16;
      one = 1;
      pin(one); pin(sig); pin(sigp); pin(sig2); pin(cnta); pin(w2c); pin(hb);
    }
    if constexpr (PART.v == 3) {
      const u32 xw = L_RED + a * 8192 + t31 * 256 + (((4 * h) ^ (t31 & 7)) * 16);
#pragma unroll
      for (int q = 0; q < 4; ++q) { xq[q] = xw ^ (q * 16); pin(xq[q]); }
    }
    if constexpr (PART.v >= 4) {
      constexpr int b = PART.v - 4;
      pin_s(jb4); pin_s(pos8);
      ab[b][0] = a0 + pos8; ab[b][1] = ab[b][0] ^ 64;
      hs[b] = hb + jb4;
      pin(ab[b][0]); pin(ab[b][1]); pin(hs[b]);
      jb4 = jb4 == 6 * 4096 ? 0 : jb4 + 4096;
      pos8 = pos8 == 6 * 8192 ? 0 : pos8 + 8192;
    }
  };
  auto setup_all = [&]() {
    setup(IC<0>{}); setup(IC<1>{}); setup(IC<2>{}); setup(IC<3>{}); setup(IC<4>{}); setup(IC<5>{});
    setup(IC<6>{}); setup(IC<7>{}); setup(IC<8>{}); setup(IC<9>{}); setup(IC<10>{});
    xr0 = L_RED + g * 8192 + (16 * c + r8) * 256 + (((2 * (lane & 7)) ^ r8) * 16); xr1 = xr0 ^ 16; sigx = (sig ^ 8) + 64;
    pin(xr0); pin(xr1); pin(sigx);
  };
  auto w2_done = [&]() {
    wait_vm<0>();
    lds_add_lane0(w2c, one);
    STAMP(6);
    __builtin_amdgcn_s_setprio(2);
  };

  if (ldr) {
    u32 w2v[2];
    if (a < 2) dma4_sc1(rws, lane == 0 ? 0u : OOR, epo, L_FLG);
    else if (a == 2) dma4(mk_rsrc(b1p, F * 2), lane * 4, w1s, L_B1);
    else dma4(mk_rsrc(b2p, D * 2), lane * 4, m * 128, L_B2);
    mk_w2v(w2v);
    setup_all();
    sched_fence();
    auto dma_g = [&](int kg) {
      const u32 kr = (kg + rot) & 7, sl = (kg % 3) * L_SLOT;
      dma16x2(rx, xv, xs + kr * 128, sl + wl);
      dma16x4(rw1, wv, w1s + kr * (64 * F * 2), sl + 8192 + 2 * wl);
    };
    auto dma_xo = [&](int kg) { dma16x2(rx, xv, (xs ^ 65536) + ((kg + rot) & 7) * 128, L_XO + kg * 8192 + wl); };
    auto dma_w2 = [&](int q) {
      int jj = q % 7 + rp;
      pin_s(jj);
      jj = jj >= 7 ? jj - 7 : jj;
      dma16x2(rw2, w2v, w2s + (q >= 7 ? (sw ^ 1) * 65536 : sw * 65536) + jj * 131072, q * 8192 + a * 2048);
    };
    constexpr int ord[14] = {9, 10, 6, 7, 8, 0, 1, 2, 3, 4, 5, 11, 12, 13};
    static_assert(N7 <= 8 && N7 + N8 <= 11, "");
    dma_g(1);
    FSTAMP(3);
    auto lit = [&](auto KG) {
      constexpr int kg = KG.v;
      wait_vm<6 + (kg == 0 ? 1 : 0) + (kg == CLR_AT + 1 ? 2 : 0)>();
      bar();
      if (kg < 6) dma_g(kg + 2);
      if (kg == CLR_AT) clear();
      if (kg == 2) tag = __builtin_amdgcn_readfirstlane(*(const LAS u32*)L_FLG) + 0x01010101u;
    };
    static_assert(CLR_AT < 5, "");
    lit(IC<0>{}); lit(IC<1>{}); lit(IC<2>{}); lit(IC<3>{}); lit(IC<4>{}); lit(IC<5>{}); lit(IC<6>{});
    dma16_sc1(rws, lv16, sts + (a & 1) * 1024, L_ST + (a & 1) * 1024);
    dma_xo(0); dma_xo(1); dma_xo(2);
    lit(IC<7>{});
    dma_xo(3); dma_xo(4); dma_xo(5); dma_xo(6); dma_xo(7);
#pragma unroll
    for (int i = 0; i < N7; ++i) dma_w2(ord[i]);
    wait_vm<2 * N7>();
    STAMP(3);
    bar();
#pragma unroll
    for (int i = N7; i < N7 + N8; ++i) dma_w2(ord[i]);
    STAMP(4);
    bar();
    if (w == 4) flag_set(tag, rws, lane == 0 ? 0u : OOR, flg + j * 4);
#pragma unroll
    for (int i = N7 + N8; i < 14; ++i) dma_w2(ord[i]);
  } else if (m == 0) {
    v4u xr[2][4];
    u32 ep, w2v[2];
#pragma unroll
    for (int k = 0; k < 8; ++k) ld16(xr[k >> 2][k & 3], rx, xv[0], rowO + w * 8192 + ((k + HROT) & 7) * 128);
    ld4_sc1(ep, rws, 0, epo);
    float sum = 0.f, sq = 0.f;
    const bf16x2 ones = {(__bf16)1.0f, (__bf16)1.0f};
    wait_vm<0>(ep);
    wait_vm4<0>(xr[0]);
    wait_vm4<0>(xr[1]);
    tag = __builtin_amdgcn_readfirstlane(ep) + 0x01010101u;
#pragma unroll
    for (int k = 0; k < 8; ++k)
#pragma unroll
      for (int e = 0; e < 4; ++e) {
        const bf16x2 v = asb2(xr[k >> 2][k & 3][e]);
        sum = __builtin_amdgcn_fdot2_f32_bf16(v, ones, sum, false);
        sq = __builtin_amdgcn_fdot2_f32_bf16(v, v, sq, false);
      }
#pragma unroll
    for (int o = 1; o < 8; o <<= 1) { sum += __shfl_xor(sum, o); sq += __shfl_xor(sq, o); }
    const float mean = sum * (1.0f / D);
    const float sd = __builtin_amdgcn_sqrtf(fmaxf(sq * (1.0f / D) - mean * mean, 0.f) + 1e-5f);
    st16_sc1<0>(v4u{asu(-mean), tag, asu(sd), tag}, rws, (lane & 7) ? OOR : (64 * kh + 8 * w + (lane >> 3)) * 16, sts);
    STAMP(1);
    if (w < 4) clear();
    else if (w == 4) dma4_sc1(rws, lane == 0 ? 0u : OOR, epo, L_FLG);
    else if (w == 5) dma4(mk_rsrc(b2p, D * 2), lane * 4, 0, L_B2);
    else if (w == 6) dma4(rws, lane < 2 ? lane * H_STRIDE : OOR, hpan - 128, L_JUNK);
    else dma4(mk_rsrc(out, 2048 * D * 2), lane == 0 ? 0u : OOR, rowO, L_JUNK);
    if (w == 4) wait_vm<0>();
    bar();
    if (w < 4) {
      wait_vm<0>();
      flag_set(tag, rws, lane == 0 ? 0u : OOR, flg + 32 + a * 4);
    } else {
      const u32 pv[4] = {lv16, lv16, lv16, lv16};
#pragma nounroll
      for (u32 i = 0; i < HELP_W2; ++i) {
        u32 rb = rg * 14 + i * 4 + a;
        rb = rb >= 56 ? rb - 56 : rb;
        dma16x4(rw2, pv, kh * 896 * 1024 + (p * 56 + rb) * 4096, L_RED + a * 4096);
      }
      mk_w2v(w2v);
#pragma nounroll
      for (int b = 0; b < 7; ++b) {
        const int jj = b + rp >= 7 ? b + rp - 7 : b + rp;
        dma16x2(rw2, w2v, w2s + sw * 65536 + jj * 131072, b * 8192 + a * 2048);
        dma16x2(rw2, w2v, w2s + (sw ^ 1) * 65536 + jj * 131072, (7 + b) * 8192 + a * 2048);
      }
    }
    setup_all();
    if (w >= 4) w2_done();
  } else {
    {
      FSTAMP(1);
      u32 xa[4], wa;
#pragma unroll
      for (int k = 0; k < 4; ++k) { xa[k] = t31 * 128 + (((2 * k + h) ^ ((t31 >> 1) & 7)) * 16); pin(xa[k]); }
      wa = 8192 + (8 * h + q4) * 256 + ((w ^ q4) * 64) + ((lane >> 4) & 1) * 32 + (lane & 3) * 8;
      pin(wa);
      const u32 selw = ((lane & 3) == 0 && ((lane >> 2) & 1) == ((lane >> 4) & 1)) ? 0x3f803f80u : 0u;
      v4u selv = {selw, selw, selw, selw};
      pin(selv);
      const bf16x8 sel = __builtin_bit_cast(bf16x8, selv);
      u32 sta = L_ST + t31 * 16 + (1 - kh) * 1024, b1a = L_B1 + w * 64 + t31 * 2;
      int hdst = hpan + (w >> 1) * (28 * 1024) + j * 4096 + (w & 1) * 2048;
      pin(sta); pin(b1a); pin_s(hdst);
      bf16x8 xf[32][2], wf[32];
      f32x16 acc[2];
      f32x4 cs;
      v4u st[2];
      v2u bfr[2];
      f32x2 kk[2], sd[2];
      u32 epl, b1w, hi2, lo2;
      float rs;
      auto rd = [&](auto N, auto PART) {
        constexpr int kg = N.v >> 2, k = N.v & 3;
        constexpr u32 sl = (kg % 3) * L_SLOT;
        if (PART.v == 0) wf[N.v] = lds_tr(wa + sl + k * 4096, wa + sl + k * 4096 + 1024);
        else {
          xf[N.v][0] = lds_b128(xa[k] + (PART.v == 1 ? sl : kg * 8192));
          xf[N.v][1] = lds_b128(xa[k] + (PART.v == 1 ? sl : kg * 8192) + 4096);
        }
      };
      auto stats = [&](auto HALF, auto PART) {
        constexpr int q = PART.v;
        if constexpr (q == 0) { st[0] = *(const LAS v4u*)sta; st[1] = *(const LAS v4u*)(sta + 512); }
        if constexpr (q == 1) {
          if (__builtin_expect(__builtin_amdgcn_ballot_w64(((st[0].y ^ tag) | (st[0].w ^ tag) | (st[1].y ^ tag) | (st[1].w ^ tag)) != 0) != 0, 0)) {
#pragma nounroll
            for (u32 spin = 0; spin < SPIN_CAP; ++spin) {
              ld16_sc1_w(st[0], rws, t31 * 16, sts + (HALF.v ^ kh ^ 1) * 1024);
              ld16_sc1_w(st[1], rws, t31 * 16, sts + (HALF.v ^ kh ^ 1) * 1024 + 512);
              if (__builtin_amdgcn_ballot_w64(((st[0].y ^ tag) | (st[0].w ^ tag) | (st[1].y ^ tag) | (st[1].w ^ tag)) != 0) == 0) break;
            }
          }
        }
        if constexpr (q >= 2 && q < 8) {
          constexpr int t = (q - 2) / 3, r = (q - 2) % 3;
          const u32 nm = st[t].x, sdv = st[t].z;
          if (r == 0) split2(asf(h ? sdv : nm), hi2, lo2);
          if (r == 1) {
            const u32 y = (hi2 & 0xffffu) | (lo2 & 0xffff0000u);
            bfr[t] = v2u{y, y};
            sd[t] = f32x2{asf(sdv), asf(sdv)};
            rs = __builtin_amdgcn_rcpf(asf(sdv));
            pin(bfr[t]); pin(sd[t]); pin(rs);
          }
          if (r == 2) { tile_consts(rs, kk[t]); pin(kk[t]); }
        }
      };
      auto gap = [&](auto N, auto S) {
        constexpr int n = N.v, kg = n >> 2, u = (n & 3) * 3 + S.v;
        sched_fence();
        if constexpr (kg < 7 && u < 8) rd(IC<4 * kg + u / 2>{}, IC<u % 2>{});
        if constexpr (kg == 7 && u < 4) { rd(IC<28 + u>{}, IC<0>{}); rd(IC<28 + u>{}, IC<1>{}); }
        if constexpr (n == 12 && S.v == 0) { epl = *(const LAS u32*)L_FLG; b1w = *(const LAS unsigned short*)b1a; }
        if constexpr (n == 19 && S.v == 0) { tag = __builtin_amdgcn_readfirstlane(epl) + 0x01010101u; b1w <<= 16; pin(b1w); }
        if constexpr (n >= 29 && 3 * (n - 29) + S.v >= 1 && 3 * (n - 29) + S.v < 8) stats(IC<0>{}, IC<3 * (n - 29) + S.v>{});
        sched_fence();
      };
      auto step = [&](auto N) {
        constexpr int n = N.v, k = n - 2;
        if constexpr (k >= 0) mfma32<k == 0>(acc[0], wf[k], xf[k][0]);
        gap(N, IC<0>{});
        if constexpr (k >= 0 && k < 28) mfma16<k == 0>(cs, sel, wf[k]);
        if constexpr (k == 28 || k == 29) { mfma16<false>(cs, sel, wf[2 * k - 28]); mfma16<false>(cs, sel, wf[2 * k - 27]); }
        gap(N, IC<1>{});
        if constexpr (k >= 0) mfma32<k == 0>(acc[1], wf[k], xf[k][1]);
        gap(N, IC<2>{});
      };
      auto iter = [&](auto KG) {
        constexpr int kg = KG.v;
        wait_lgkm0();
        bar();
        if (kg == 0) STAMP(1);
        FSTAMP(2 + kg);
        if (kg == 7) stats(IC<0>{}, IC<0>{});
        step(IC<4 * kg>{}); step(IC<4 * kg + 1>{}); step(IC<4 * kg + 2>{}); step(IC<4 * kg + 3>{});
      };
      iter(IC<0>{}); iter(IC<1>{}); iter(IC<2>{}); iter(IC<3>{}); iter(IC<4>{}); iter(IC<5>{}); iter(IC<6>{}); iter(IC<7>{});
      FSTAMP(10);
      step(IC<32>{});
      asm volatile("s_nop 4" : "+v"(cs));
      mfma32<false>(acc[0], wf[31], xf[31][0]);
      split2(h ? asf(b1w) : cs[0], hi2, lo2);
      v2u afr = {hi2, lo2};
      pin(afr);
      sched_fence();
      mfma32<false>(acc[1], wf[31], xf[31][1]);
      __builtin_amdgcn_s_setprio(3);
      auto finish = [&](auto HALF) {
        mfma32k8(acc[0], afr, bfr[0]);
        mfma32k8(acc[1], afr, bfr[1]);
        nop11(acc[0], acc[1]);
        STAMP(2 + 2 * HALF.v);
        v4u o = gelu8<0>(acc[0], kk[0], sd[0]);
        st16_sc1<0>(o, rws, lv16, hdst + HALF.v * (112 * 1024));
        o = gelu8<1>(acc[0], kk[0], sd[0]);
        st16_sc1<1024>(o, rws, lv16, hdst + HALF.v * (112 * 1024));
        o = gelu8<0>(acc[1], kk[1], sd[1]);
        st16_sc1<0>(o, rws, lv16, hdst + HALF.v * (112 * 1024) + 56 * 1024);
        o = gelu8<1>(acc[1], kk[1], sd[1]);
        st16_sc1<1024>(o, rws, lv16, hdst + HALF.v * (112 * 1024) + 56 * 1024);
        STAMP(3 + 2 * HALF.v);
      };
      FSTAMP(11);
      finish(IC<0>{});
#pragma unroll
      for (int k = 0; k < 4; ++k) { xa[k] += L_XO; pin(xa[k]); }
      sta ^= 1024;
      pin(sta);
      bar();
      FSTAMP(12);
      rd(IC<0>{}, IC<2>{}); rd(IC<1>{}, IC<2>{}); rd(IC<2>{}, IC<2>{});
      stats(IC<1>{}, IC<0>{});
      auto step2 = [&](auto N) {
        constexpr int n = N.v;
        if constexpr (n == S9) { wait_vm<0>(); bar(); FSTAMP(13); }
        mfma32<n == 0>(acc[0], wf[n], xf[n][0]);
        sched_fence();
        if constexpr (n + 3 < 32) rd(IC<n + 3>{}, IC<2>{});
        if (S2PAD >= 0) asm volatile("s_nop %0" ::"i"(S2PAD));
        sched_fence();
        mfma32<n == 0>(acc[1], wf[n], xf[n][1]);
        sched_fence();
        if constexpr (n >= 2 && n < 9) stats(IC<1>{}, IC<n - 1>{});
        if constexpr (n >= 10 && n < 32 && (n & 1) == 0) setup(IC<(n - 10) / 2>{});
        sched_fence();
      };
      static_assert(S9 >= 12 && S9 < 32, "");
      step2(IC<0>{}); step2(IC<1>{}); step2(IC<2>{}); step2(IC<3>{}); step2(IC<4>{}); step2(IC<5>{}); step2(IC<6>{}); step2(IC<7>{});
      step2(IC<8>{}); step2(IC<9>{}); step2(IC<10>{}); step2(IC<11>{}); step2(IC<12>{}); step2(IC<13>{}); step2(IC<14>{}); step2(IC<15>{});
      step2(IC<16>{}); step2(IC<17>{}); step2(IC<18>{}); step2(IC<19>{}); step2(IC<20>{}); step2(IC<21>{}); step2(IC<22>{}); step2(IC<23>{});
      step2(IC<24>{}); step2(IC<25>{}); step2(IC<26>{}); step2(IC<27>{}); step2(IC<28>{}); step2(IC<29>{}); step2(IC<30>{}); step2(IC<31>{});
      finish(IC<1>{});
      *(volatile LAS u32*)(sig + 128) = one;
      u32 old = 0;
      wait_vm<0>();
      lds_inc_lane0(old, cnta, one);
      lgkm0_pin(old);
      if (__builtin_amdgcn_readfirstlane(old) == 3)
        flag_set(tag, rws, lane == 0 ? 0u : OOR, flg + 256 + j * 4);
      __builtin_amdgcn_s_setprio(0);
      STAMP(6);
    }
  }

  {
    v4u hf[28];
    auto ld_h = [&](auto I) { ld16_sc1<(I.v & 3) * 1024>(hf[I.v], rws, hs[I.v >> 2], 0); };
    bf16x8 wb[28][2];
    auto rd_w = [&](auto I, auto N) {
      const u32 o = ab[I.v >> 2][N.v] + (I.v & 3) * 2048;
      wb[I.v][N.v] = lds_tr(o, o + 1024);
    };
    auto flags = [&]() {
      if (a == 0) {
        if (hh) lds_wait<1, 1>(sigp);
        flag_wait<POLL_SLEEP>(rws, pollv, hh ? (m ? 0xffu : 0x7ffu) : 0x7fu, tag);
        *(volatile LAS u32*)(cnta + 8 + hh * 4) = one;
      } else {
        lds_wait<1, 1>(cnta + 8 + hh * 4);
      }
    };
    if (m == 0) flags();
    else if (hh) lds_wait<1, 4>(cnta);
    STAMP(7);
    ld_h(IC<0>{}); ld_h(IC<1>{}); ld_h(IC<2>{}); ld_h(IC<3>{});
    if (m != 0) {
      flags();
      if (hh == 0) w2_done();
    }
    STAMP(8);
    constexpr int WIN = WINDOW;
    ld_h(IC<4>{}); ld_h(IC<5>{}); ld_h(IC<6>{}); ld_h(IC<7>{});
    if constexpr (WIN > 8) { ld_h(IC<8>{}); ld_h(IC<9>{}); }
    if constexpr (WIN > 10) { ld_h(IC<10>{}); ld_h(IC<11>{}); }
    if constexpr (WIN > 12) { ld_h(IC<12>{}); ld_h(IC<13>{}); }
    if constexpr (WIN > 14) { ld_h(IC<14>{}); ld_h(IC<15>{}); }
    if constexpr (WIN > 16) { ld_h(IC<16>{}); ld_h(IC<17>{}); ld_h(IC<18>{}); ld_h(IC<19>{}); }
    if constexpr (WIN > 20) { ld_h(IC<20>{}); ld_h(IC<21>{}); ld_h(IC<22>{}); ld_h(IC<23>{}); }
    if constexpr (WIN > 24) { ld_h(IC<24>{}); ld_h(IC<25>{}); ld_h(IC<26>{}); ld_h(IC<27>{}); }
    static_assert(WIN == 24 || WIN == 28 || WIN == 8 || WIN == 10 || WIN == 12 || WIN == 14 || WIN == 16 || WIN == 20, "");
    lds_wait<1, 4>(w2c);
    rd_w(IC<0>{}, IC<1>{}); rd_w(IC<0>{}, IC<0>{}); rd_w(IC<1>{}, IC<1>{}); rd_w(IC<1>{}, IC<0>{});
    if (m != 0 && hh == 0) lds_wait<1, 1>(L_FLG + 224 + (lane & 3) * 4);
    if (hh == 0) STAMP(2);
    f32x16 acc0, acc1;
    auto gstamp = [&](auto K) { GSTAMP(K.v); };
    auto g2 = [&](auto I) {
      constexpr int i = I.v;
      wait_vm<(i + WIN < 28 ? WIN - 1 : 27 - i)>(hf[i]);
      mfma32<i == 0>(acc1, wb[i][1], __builtin_bit_cast(bf16x8, hf[i]));
      if constexpr ((i & 3) == 0) gstamp(IC<1 + i / 4>{});
      sched_fence();
      if constexpr (i + WIN < 28) ld_h(IC<i + WIN>{});
      if constexpr (i == GOAT) *(volatile LAS u32*)sig = one;
      if constexpr (i == 1) *(volatile LAS u32*)(sig + 32) = one;
      if constexpr (i == 22) fr = *(volatile LAS u32*)(sig2 + 96);
      if constexpr (ARR == 0 || i + WIN >= 28) rd_w(IC<i + 2>{}, IC<1>{});
      sched_fence();
      if (PAD1 >= 0) asm volatile("s_nop %0" ::"i"(PAD1));
      mfma32<i == 0>(acc0, wb[i][0], __builtin_bit_cast(bf16x8, hf[i]));
      sched_fence();
      if constexpr (ARR == 1 && i + WIN < 28) rd_w(IC<i + 2>{}, IC<1>{});
      rd_w(IC<i + 2>{}, IC<0>{});
      sched_fence();
      if (PAD2 >= 0) asm volatile("s_nop %0" ::"i"(PAD2));
    };
    g2(IC<0>{}); g2(IC<1>{}); g2(IC<2>{}); g2(IC<3>{}); g2(IC<4>{}); g2(IC<5>{}); g2(IC<6>{});
    g2(IC<7>{}); g2(IC<8>{}); g2(IC<9>{}); g2(IC<10>{}); g2(IC<11>{}); g2(IC<12>{}); g2(IC<13>{});
    g2(IC<14>{}); g2(IC<15>{}); g2(IC<16>{}); g2(IC<17>{}); g2(IC<18>{}); g2(IC<19>{}); g2(IC<20>{});
    g2(IC<21>{}); g2(IC<22>{}); g2(IC<23>{});
    auto tail1 = [&](auto I) {
      wait_vm<27 - I.v>(hf[I.v]);
      mfma32<false>(acc1, wb[I.v][1], __builtin_bit_cast(bf16x8, hf[I.v]));
      sched_fence();
      if constexpr (I.v < 26) rd_w(IC<I.v + 2>{}, IC<1>{}); else rd_w(I, IC<0>{});
      sched_fence();
    };
    tail1(IC<24>{}); tail1(IC<25>{}); tail1(IC<26>{}); tail1(IC<27>{});
    mfma32<false>(acc0, wb[24][0], __builtin_bit_cast(bf16x8, hf[24]));
    if (hh && __builtin_amdgcn_ballot_w64(fr != 1) != 0) lds_wait<0, 1>(sig2 + 96);
    mfma32<false>(acc0, wb[25][0], __builtin_bit_cast(bf16x8, hf[25]));
    asm volatile("s_nop 7" : "+v"(acc1));
    auto wr = [&](const f32x16& v, int q, u32 off) { *(LAS f32x4*)(xq[q] + off) = f32x4{v[4 * q], v[4 * q + 1], v[4 * q + 2], v[4 * q + 3]}; };
    mfma32<false>(acc0, wb[26][0], __builtin_bit_cast(bf16x8, hf[26]));
    sched_fence();
    wr(acc1, 0, 128); wr(acc1, 1, 128);
    sched_fence();
    mfma32<false>(acc0, wb[27][0], __builtin_bit_cast(bf16x8, hf[27]));
    sched_fence();
    wr(acc1, 2, 128); wr(acc1, 3, 128);
    STAMP(9);
    asm volatile("s_nop 9" : "+v"(acc0)::"memory");
    wr(acc0, 0, 0); wr(acc0, 1, 0); wr(acc0, 2, 0); wr(acc0, 3, 0);
    asm volatile("" ::: "memory");
    *(volatile LAS u32*)(sig + 64) = one;
    STAMP(10);
    if (hh == 0) {
      f32x4 sum[4];
      auto gather = [&]() {
        f32x4 u[4], v[4];
#pragma unroll
        for (int q = 0; q < 4; ++q) {
          u[q] = lds_f4(((q & 1) ? xr1 : xr0) + (q >> 1) * 2048);
          v[q] = lds_f4(((q & 1) ? xr1 : xr0) + (q >> 1) * 2048 + 16384);
        }
#pragma unroll
        for (int q = 0; q < 4; ++q) sum[q] = add4(u[q], v[q]);
      };
      lds_wait<0, 1>(sigx);
      gather();
      asm volatile("" ::: "memory");
      *(volatile LAS u32*)(sig + 96) = one;
      v4u d[2];
#pragma unroll
      for (int q = 0; q < 4; ++q) {
        d[q >> 1][2 * (q & 1)] = min(pack2(sum[q][0], sum[q][1]), 0xfffffffeu);
        d[q >> 1][2 * (q & 1) + 1] = min(pack2(sum[q][2], sum[q][3]), 0xfffffffeu);
      }
      pin(d[0]); pin(d[1]);
      st16_sc1<0>(d[0], rws, lv16, pdst); st16_sc1<1024>(d[1], rws, lv16, pdst);
      STAMP(11);
      __builtin_amdgcn_s_setprio(0);
      const u32 psrc = WS_P + ((pk ^ 1) * 8 + m) * P_STRIDE + a * 2048;
      const v4u bb = *(const LAS v4u*)(L_B2 + (lane & 7) * 16);
      lds_wait<1, 1>(sigp + 32);
      wait_vm<0>();
      if (DP) __builtin_amdgcn_s_sleep(DP);
      ld16_sc1<0>(d[0], rws, lv16, psrc); ld16_sc1<1024>(d[1], rws, lv16, psrc);
      STAMP(12);
      const u32 b0 = bb[0], b1 = bb[1], b2 = bb[2], b3 = bb[3];
      f32x4 bl = {bf_lo(b0), bf_hi(b0), bf_lo(b1), bf_hi(b1)}, bh = {bf_lo(b2), bf_hi(b2), bf_lo(b3), bf_hi(b3)};
      u32 ov = (32 * g + 16 * c + r8) * 1024 + (lane & 7) * 16;
      pin(bl); pin(bh); pin(ov);
      wait_vm<0>(d[0]);
      pin(d[1]);
      auto unsent = [&]() {
        const u32 mx = max(max(max(d[0][0], d[0][1]), max(d[0][2], d[0][3])), max(max(d[1][0], d[1][1]), max(d[1][2], d[1][3])));
        return __builtin_amdgcn_ballot_w64(mx == ~0u) != 0;
      };
      if (__builtin_expect(unsent(), 0)) {
#pragma nounroll
        for (u32 spin = 0; spin < SPIN_CAP; ++spin) {
          __builtin_amdgcn_s_sleep(5);
          ld32_sc1_w(d, rws, lv16, psrc);
          if (!unsent()) break;
        }
      }
      STAMP(5);
      f32x4 pb[4];
#pragma unroll
      for (int q = 0; q < 4; ++q) {
        const u32 p0 = d[q >> 1][2 * (q & 1)], p1 = d[q >> 1][2 * (q & 1) + 1];
        pb[q] = add4(f32x4{bf_lo(p0), bf_hi(p0), bf_lo(p1), bf_hi(p1)}, (q & 1) ? bh : bl);
      }
      lds_wait<WSL, 1>(sig2 + 64);
      v4u o[2];
      {
        f32x4 u[4], v[4];
#pragma unroll
        for (int q = 0; q < 4; ++q) {
          u[q] = lds_f4(((q & 1) ? xr1 : xr0) + (q >> 1) * 2048);
          v[q] = lds_f4(((q & 1) ? xr1 : xr0) + (q >> 1) * 2048 + 16384);
        }
#pragma unroll
        for (int q = 0; q < 4; ++q) {
          const f32x4 r = (pb[q] + u[q]) + v[q];
          o[q >> 1][2 * (q & 1)] = pack2(r[0], r[1]);
          o[q >> 1][2 * (q & 1) + 1] = pack2(r[2], r[3]);
        }
      }
      STAMP(13);
      const v4u ro = mk_rsrc(out, 2048 * D * 2);
      st16_out(o[0], ro, ov, rowO + m * 128);
      st16_out(o[1], ro, ov, rowO + m * 128 + 8 * 1024);
      if (kh == 0 && m == 0 && w == 4) st4_sc1(tag, rws, lane == 0 ? 0u : OOR, epo);
    }
  }
}

extern "C" void kernel_launch(void* const* d_in, const int* in_sizes, int n_in, void* d_out, int out_size, void* d_ws,
                              size_t ws_size, hipStream_t stream) {
  (void)in_sizes; (void)n_in; (void)out_size; (void)ws_size;
  (void)hipFuncSetAttribute((const void*)mixer_mlp, hipFuncAttributeMaxDynamicSharedMemorySize, L_BYTES);
  mixer_mlp<<<256, 512, L_BYTES, stream>>>((const char*)d_in[0], (const char*)d_in[1], (const char*)d_in[2], (const char*)d_in[3],
                                           (const char*)d_in[4], (char*)d_out, (char*)d_ws, (char*)d_ws);
}
```
